```python
import torch

def _softmax(x, axis=-1):
    return torch.softmax(x, dim=axis)

def _gelu(x):
    return torch.nn.functional.gelu(x, approximate='tanh')

def _silu(x):
    return torch.nn.functional.silu(x)

def _sigmoid(x):
    return torch.sigmoid(x)

def _layernorm(x, axis=-1, eps=1e-05):
    m = x.mean(dim=axis, keepdim=True)
    v = x.var(dim=axis, keepdim=True, unbiased=False)
    return (x - m) / torch.sqrt(v + eps)

def _scatter_add(tgt, idx, src, axis=0):
    return tgt.clone().index_add_(axis, idx, src)

def _segment_sum(src, seg, n):
    out = torch.zeros((n,) + src.shape[1:], dtype=src.dtype)
    return out.index_add_(0, seg, src)

def _pad(x, pw):
    flat = tuple((v for pair in reversed(pw) for v in pair))
    return torch.nn.functional.pad(x, flat)
kernel_name = 'interleave_layers__memory_aug_recurrent_mem_tr__mixer_channel_mlp__prompt'

def setup_inputs():

    _w = lambda a: (lambda t: t.to(torch.bfloat16) if t.is_floating_point() else t)(torch.as_tensor(a))

    def _A_setup():
        B, S, Dm = (1, 2048, 1024)
        H, Dh = (32, 128)
        M, seg = (8, 32)
        seg = min(seg, S)
        S = S // seg * seg
        return {'x': _w((lambda *s: torch.randn(*s, dtype=torch.bfloat16) * 0.05)(B, S, Dm)), 'mem0': _w((lambda *s: torch.randn(*s, dtype=torch.bfloat16) * 0.8)(B, M, Dm)), 'Wq': _w((lambda *s: torch.randn(*s, dtype=torch.bfloat16) * 1.6)(Dm, H, Dh) * Dm ** (-0.5)), 'Wk': _w((lambda *s: torch.randn(*s, dtype=torch.bfloat16) * 1.6)(Dm, H, Dh) * Dm ** (-0.5)), 'Wv': _w((lambda *s: torch.randn(*s, dtype=torch.bfloat16) * 0.8)(Dm, H, Dh) * Dm ** (-0.5)), 'Wo': _w((lambda *s: torch.randn(*s, dtype=torch.bfloat16) * 0.8)(H, Dh, Dm) * (H * Dh) ** (-0.5)), 'seg_len': seg}
    _A_in = _A_setup()

    def _B_setup():
        B, S, D, F = (1, 2048, 1024, 3584)
        return {'x': _w((lambda *s: torch.randn(*s, dtype=torch.bfloat16) * 0.05)(B, S, D)), 'w1': _w((lambda *s: torch.randn(*s, dtype=torch.bfloat16) * 3.2)(D, F) * D ** (-0.5)), 'b1': _w((lambda *s: torch.randn(*s, dtype=torch.bfloat16) * 0.8)(F)), 'w2': _w((lambda *s: torch.randn(*s, dtype=torch.bfloat16) * 0.1)(F, D) * F ** (-0.5)), 'b2': _w((lambda *s: torch.randn(*s, dtype=torch.bfloat16) * 0.1)(D))}
    _B_in = _B_setup()
    return {'A_x': _A_in['x'], 'A_mem0': _A_in['mem0'], 'A_Wq': _A_in['Wq'], 'A_Wk': _A_in['Wk'], 'A_Wv': _A_in['Wv'], 'A_Wo': _A_in['Wo'], 'A_seg_len': _A_in['seg_len'], 'B_x': _B_in['x'], 'B_w1': _B_in['w1'], 'B_b1': _B_in['b1'], 'B_w2': _B_in['w2'], 'B_b2': _B_in['b2']}

def reference(A_x, A_mem0, A_Wq, A_Wk, A_Wv, A_Wo, A_seg_len, B_x, B_w1, B_b1, B_w2, B_b2):

    def _A_ref(x, mem0, Wq, Wk, Wv, Wo, seg_len):
        B, S, Dm = x.shape
        H, Dh = (Wq.shape[1], Wq.shape[2])
        M = mem0.shape[1]
        scale = Dh ** (-0.5)
        n_seg = S // seg_len
        L = M + seg_len
        idx = torch.arange(L)
        mask = (idx[:, None] < M) | (idx[:, None] >= idx[None, :])
        mem = mem0
        outs = []
        for s in range(n_seg):
            xs = x[:, s * seg_len:(s + 1) * seg_len, :].clone()
            h = torch.concatenate([mem, xs], axis=1)
            q = torch.einsum('btd,dhe->bhte', h, Wq)
            k = torch.einsum('btd,dhe->bhte', h, Wk)
            v = torch.einsum('btd,dhe->bhte', h, Wv)
            scores = torch.einsum('bhqe,bhke->bhqk', q, k) * scale
            scores = torch.where(mask[None, None], scores, -1000000000.0)
            p = _softmax(scores, axis=-1)
            o = torch.einsum('bhqk,bhke->bhqe', p, v)
            o = torch.einsum('bhte,hed->btd', o, Wo)
            mem = o[:, :M, :].clone()
            outs.append(o[:, M:, :])
        return torch.concatenate(outs, axis=1)

    def _B_ref(x, w1, b1, w2, b2):
        y = _layernorm(x, axis=-1)
        h = _gelu(torch.einsum('bsd,df->bsf', y, w1) + b1)
        return torch.einsum('bsf,fd->bsd', h, w2) + b2
    x = A_x
    x = x + _A_ref(_layernorm(x, axis=-1), A_mem0, A_Wq, A_Wk, A_Wv, A_Wo, A_seg_len)
    x = x + _B_ref(_layernorm(x, axis=-1), B_w1, B_b1, B_w2, B_b2)
    x = x + _A_ref(_layernorm(x, axis=-1), A_mem0, A_Wq, A_Wk, A_Wv, A_Wo, A_seg_len)
    x = x + _B_ref(_layernorm(x, axis=-1), B_w1, B_b1, B_w2, B_b2)
    return x
```

```cpp
#include <hip/hip_runtime.h>
#include <hip/hip_bf16.h>
#include <stdint.h>
#define CAL_ME 0

namespace {
constexpr int S_TOK = 2048, DM = 1024, NH = 32, DH = 128, HD = NH * DH;
constexpr int MEMR = 8, SEG = 32, NSEG = S_TOK / SEG;
constexpr int FF = 3584;
constexpr int NQKV = 3 * HD;

typedef unsigned short bf16_t;
typedef short bf16x8 __attribute__((ext_vector_type(8)));
typedef float f32x4 __attribute__((ext_vector_type(4)));
typedef unsigned v4u __attribute__((ext_vector_type(4)));
typedef unsigned v2u __attribute__((ext_vector_type(2)));
#define LAS __attribute__((address_space(3)))

constexpr int AUX_SC1 = 16;

__device__ __forceinline__ float bf2f(unsigned short b) { return __builtin_bit_cast(float, (unsigned)b << 16); }
__device__ __forceinline__ unsigned short f2bf(float f) {
  __hip_bfloat16 h = __float2bfloat16(f);
  return __builtin_bit_cast(unsigned short, h);
}
__device__ __forceinline__ float rbf(float f) { return bf2f(f2bf(f)); }
__device__ __forceinline__ unsigned pack2(float lo, float hi) { return (unsigned)f2bf(lo) | ((unsigned)f2bf(hi) << 16); }

constexpr int NCHUNK = 8;
constexpr int NBLK = 16;
constexpr int OSPLIT = 4, NSPLIT = 4;
constexpr int CROWS = 8;
constexpr int CTASKS = 128 / CROWS;
enum { K_Q = 0, K_T = 1, K_O = 2, K_C1 = 3, K_M = 4, K_N = 5, K_C2 = 6, K_CH = 7, K_QQ = 8, K_TR = 9 };
constexpr int FSPLIT = 8;
static_assert((FF / FSPLIT) % 64 == 0 && (HD / FSPLIT) % 64 == 0, "fine split");
constexpr int OSPLIT2 = 2, NSPLIT2 = 2;

constexpr size_t al256(size_t x) { return (x + 255) & ~(size_t)255; }
constexpr int CH_WGS = 128;
constexpr int CH_MAXSTEP = 128;
constexpr size_t CT_TMO = 0, CT_HEAD = 256, CT_CNT = 32768;
constexpr size_t C_CTL = 24576;
constexpr int CT_FAST = 320;
constexpr size_t C_ZEND = CT_CNT + (size_t)360 * 4096;
constexpr size_t C_VOTE = C_ZEND + 4096;
constexpr size_t C_PAGE = (size_t)2 << 20;
constexpr size_t C_XP = C_PAGE;
constexpr size_t XM_SLOT = 8 * 128 * 256;
constexpr size_t C_XM = 2 * C_PAGE;
constexpr size_t XQ_PAR = 128 * 4096;
constexpr size_t C_XQ = C_XM + 3 * XM_SLOT;
constexpr size_t C_END = 3 * C_PAGE;
static_assert(C_VOTE + 64 <= C_PAGE && C_XQ + 2 * XQ_PAR <= C_END, "exchange buffers");
constexpr size_t WS_WQKVT = al256(C_END);
constexpr size_t WS_WOT = WS_WQKVT + (size_t)NQKV * DM * 2;
constexpr size_t WS_W1T = WS_WOT + (size_t)DM * HD * 2;
constexpr size_t WS_W2T = WS_W1T + (size_t)FF * DM * 2;
constexpr size_t WS_XN = WS_W2T + (size_t)DM * FF * 2;
constexpr size_t WS_QKV = WS_XN + (size_t)S_TOK * DM * 2;
constexpr size_t KVM_LAYER = (size_t)NSEG * 2 * MEMR * HD * 2;
constexpr size_t WS_KVM = WS_QKV + (size_t)S_TOK * NQKV * 2;
constexpr size_t WS_O = WS_KVM + 2 * KVM_LAYER;
constexpr size_t WS_X1 = WS_O + (size_t)S_TOK * HD * 2;
constexpr size_t WS_XN1 = WS_X1 + (size_t)S_TOK * DM * 2;
constexpr size_t WS_H = WS_XN1 + (size_t)S_TOK * DM * 2;
constexpr size_t WS_X2 = WS_H + (size_t)S_TOK * FF * 2;
constexpr size_t WS_XNB = WS_X2 + (size_t)S_TOK * DM * 2;
constexpr size_t WS_SLAB = WS_XNB + (size_t)S_TOK * DM * 2;
constexpr size_t SLAB_CHUNK = (size_t)8 * 256 * 1024 * 4;
constexpr size_t WS_END = WS_SLAB + NCHUNK * SLAB_CHUNK;
static_assert(WS_END + C_PAGE <= ((size_t)256 << 20), "workspace");

struct TrJob { const bf16_t* src; bf16_t* dst; int R; int C; int tile0; int pad; };
struct PreParams { TrJob j[2]; const bf16_t* x; bf16_t* xn; unsigned char* ws; int ntr; int pad; };
constexpr int PRE_LN = S_TOK / 4;
constexpr int PRE_CAL = 8;
constexpr int PRE_FZ = (int)(C_ZEND / 16384), PRE_FF = (int)(XM_SLOT / 16384);
static_assert(C_ZEND % 16384 == 0 && XM_SLOT % 16384 == 0, "fill granularity");

__global__ void __launch_bounds__(256) k_pre(PreParams p) {
  __shared__ bf16_t t[64][72];
  int b = blockIdx.x;
  const int tid = threadIdx.x;
  if (b < PRE_CAL) {
    const int c = tid >> 6;
    unsigned* lat = (unsigned*)&t[0][0];
#pragma unroll 1
    for (int pg = 0; pg < 2; ++pg) {
      unsigned best = 0xFFFFFFFFu;
#pragma unroll 1
      for (int k = 0; k < 6; ++k) {
        unsigned* a = (unsigned*)(p.ws + (pg ? C_XQ : C_XP) + (size_t)(b * 6 + k) * 16384 + c * 4096);
        const unsigned long long t0 = __builtin_amdgcn_s_memtime();
        __builtin_amdgcn_s_waitcnt(0xC07F);
        __hip_atomic_store(a + (tid & 63), 0u, __ATOMIC_RELAXED, __HIP_MEMORY_SCOPE_AGENT);
        asm volatile("s_waitcnt vmcnt(0)" ::: "memory");
        const unsigned long long t1 = __builtin_amdgcn_s_memtime();
        __builtin_amdgcn_s_waitcnt(0xC07F);
        const unsigned d = (unsigned)(t1 - t0);
        best = d < best ? d : best;
      }
      if ((tid & 63) == 0) lat[pg * 4 + c] = best;
    }
    __syncthreads();
    if (tid < 2) {
      const unsigned xcc = (unsigned)__builtin_amdgcn_s_getreg((3 << 11) | 20) & 7u;
      const unsigned* l = lat + tid * 4;
      const unsigned lo = l[0] < l[1] ? l[0] : l[1], hi = l[2] < l[3] ? l[2] : l[3];
      *(unsigned*)(p.ws + C_VOTE + b * 8 + tid * 4) = (hi < lo ? 2u : 0u) ^ (xcc & 4 ? 2u : 0u);
    }
    return;
  }
  b -= PRE_CAL;
  if (b < p.ntr) {
    int ji = 0;
    if (b >= p.j[1].tile0) ji = 1;
    const TrJob J = p.j[ji];
    const int tb = b - J.tile0, tcn = J.C / 64, tr = tb / tcn, tc = tb % tcn;
#pragma unroll
    for (int i = 0; i < 2; ++i) {
      const int idx = tid + i * 256, r = idx >> 3, c8 = idx & 7;
      const v4u v = *(const v4u*)(J.src + (size_t)(tr * 64 + r) * J.C + tc * 64 + c8 * 8);
      *(v4u*)&t[r][c8 * 8] = v;
    }
    __syncthreads();
#pragma unroll
    for (int i = 0; i < 2; ++i) {
      const int idx = tid + i * 256, c = idx >> 3, r8 = idx & 7;
      bf16_t o[8];
#pragma unroll
      for (int k = 0; k < 8; ++k) o[k] = t[r8 * 8 + k][c];
      *(v4u*)(J.dst + (size_t)(tc * 64 + c) * J.R + tr * 64 + r8 * 8) = *(v4u*)o;
    }
    return;
  }
  b -= p.ntr;
  if (b < PRE_LN) {
    const int row = b * 4 + (tid >> 6), lane = tid & 63;
    const bf16_t* xr = p.x + (size_t)row * DM;
    v4u a = *(const v4u*)(xr + lane * 8), bb = *(const v4u*)(xr + 512 + lane * 8);
    float f[16];
#pragma unroll
    for (int i = 0; i < 4; ++i) { f[2 * i] = bf2f(a[i] & 0xffff); f[2 * i + 1] = bf2f(a[i] >> 16); f[8 + 2 * i] = bf2f(bb[i] & 0xffff); f[8 + 2 * i + 1] = bf2f(bb[i] >> 16); }
    float s = 0.f;
#pragma unroll
    for (int i = 0; i < 16; ++i) s += f[i];
#pragma unroll
    for (int m = 32; m >= 1; m >>= 1) s += __shfl_xor(s, m);
    const float mean = s * (1.0f / DM);
    float q = 0.f;
#pragma unroll
    for (int i = 0; i < 16; ++i) { const float d = f[i] - mean; q += d * d; }
#pragma unroll
    for (int m = 32; m >= 1; m >>= 1) q += __shfl_xor(q, m);
    const float mb = rbf(mean), vb = rbf(q * (1.0f / DM));
    const float den = rbf(sqrtf(rbf(vb + 1e-5f)));
    v4u oa, ob;
#pragma unroll
    for (int i = 0; i < 4; ++i) {
      oa[i] = pack2(rbf(f[2 * i] - mb) / den, rbf(f[2 * i + 1] - mb) / den);
      ob[i] = pack2(rbf(f[8 + 2 * i] - mb) / den, rbf(f[8 + 2 * i + 1] - mb) / den);
    }
    bf16_t* yr = p.xn + (size_t)row * DM;
    *(v4u*)(yr + lane * 8) = oa; *(v4u*)(yr + 512 + lane * 8) = ob;
    return;
  }
  b -= PRE_LN;
  const unsigned v = b < PRE_FZ ? 0u : 0xFFFFFFFFu;
  unsigned char* d = b < PRE_FZ ? p.ws + (size_t)b * 16384 : p.ws + C_XM + (size_t)(b - PRE_FZ) * 16384;
#pragma unroll
  for (int i = 0; i < 4; ++i) *(v4u*)(d + i * 4096 + tid * 16) = (v4u){v, v, v, v};
}

struct MegaP {
  const bf16_t* x_in; const bf16_t* mem0; const bf16_t* b1; const bf16_t* b2; const bf16_t* w1; const bf16_t* w2; const bf16_t* wq; const bf16_t* wo;
  bf16_t* out; unsigned char* ws;
};
typedef __amdgpu_buffer_rsrc_t rsrc_t;
__device__ __forceinline__ v4u ld16(rsrc_t rs, unsigned off) { return __builtin_amdgcn_raw_buffer_load_b128(rs, (int)off, 0, AUX_SC1); }
__device__ __forceinline__ v2u ld8(rsrc_t rs, unsigned off) { return __builtin_amdgcn_raw_buffer_load_b64(rs, (int)off, 0, AUX_SC1); }
__device__ __forceinline__ void st16(rsrc_t rs, unsigned off, v4u v) { __builtin_amdgcn_raw_buffer_store_b128(v, rs, (int)off, 0, AUX_SC1); }
__device__ __forceinline__ void st8(rsrc_t rs, unsigned off, v2u v) { __builtin_amdgcn_raw_buffer_store_b64(v, rs, (int)off, 0, AUX_SC1); }
__device__ __forceinline__ unsigned* cnt_ptr(unsigned char* ws, int kind, int L, int c) { return (unsigned*)(ws + CT_CNT + (size_t)((kind * 2 + L) * 16 + c) * 4096 + (blockIdx.x & 15) * 256); }
__device__ __forceinline__ unsigned cnt_off(int kind, int L, int c) { return (unsigned)(CT_CNT + ((kind * 2 + L) * 16 + c) * 4096); }
__device__ __forceinline__ unsigned cntf_off(int kind, int sg) { return (unsigned)(CT_CNT + (CT_FAST + kind * 4 + sg) * 4096); }
__device__ __forceinline__ unsigned* cntf_ptr(unsigned char* ws, int kind, int sg) { return (unsigned*)(ws + cntf_off(kind, sg) + (blockIdx.x & 15) * 256); }
template <int N> __device__ __forceinline__ unsigned urow_ror(unsigned x) { return (unsigned)__builtin_amdgcn_update_dpp(0, (int)x, 0x120 + N, 0xf, 0xf, false); }
__device__ __forceinline__ unsigned row16_sum_u(unsigned x) { x += urow_ror<8>(x); x += urow_ror<4>(x); x += urow_ror<2>(x); return x + urow_ror<1>(x); }
__device__ __forceinline__ unsigned cnt_read(rsrc_t rs, unsigned off, int lane) {
  unsigned x = __builtin_amdgcn_raw_buffer_load_b32(rs, (int)(off + (lane & 15) * 256), 0, AUX_SC1);
  x += urow_ror<8>(x); x += urow_ror<4>(x); x += urow_ror<2>(x); return x + urow_ror<1>(x);
}

constexpr int LAG = 14;
constexpr int NTASKS = 6040, NTASK0 = 6040, WPOOL1 = 0;
__constant__ const unsigned kTasks[NTASKS] = {
  0x02600,0x02601,0x02602,0x02200,0x02201,0x02202,0x02203,0x02204,0x02205,0x02206,0x02207,0x02208,0x02209,0x0220a,0x00000,0x00001,
  0x00002,0x00003,0x00004,0x00005,0x00006,0x00007,0x00008,0x00009,0x0000a,0x0000b,0x0000c,0x0000d,0x0000e,0x0000f,0x00010,0x00011,
  0x00012,0x00013,0x00014,0x00015,0x00016,0x00017,0x00018,0x00019,0x0001a,0x0001b,0x0001c,0x0001d,0x0001e,0x0001f,0x00020,0x00021,
  0x00022,0x00023,0x00024,0x00025,0x00026,0x00027,0x00028,0x00029,0x0002a,0x0002b,0x0002c,0x0002d,0x0002e,0x0002f,0x00030,0x00031,
  0x00032,0x00033,0x00034,0x00035,0x00036,0x00037,0x00038,0x00039,0x0003a,0x0003b,0x0003c,0x0003d,0x0003e,0x0003f,0x90000,0x90001,
  0x90002,0x90003,0x90004,0x90005,0x90006,0x90007,0x90008,0x90009,0x9000a,0x9000b,0x9000c,0x9000d,0x9000e,0x9000f,0x90010,0x90011,
  0x90012,0x90013,0x90014,0x90015,0x90016,0x90017,0x90018,0x90019,0x9001a,0x9001b,0x9001c,0x9001d,0x9001e,0x9001f,0x90020,0x90021,
  0x90022,0x90023,0x90024,0x90025,0x90026,0x90027,0x90028,0x90029,0x9002a,0x9002b,0x9002c,0x9002d,0x9002e,0x9002f,0x90030,0x90031,
  0x90032,0x90033,0x90034,0x90035,0x90036,0x90037,0x90038,0x90039,0x9003a,0x9003b,0x9003c,0x9003d,0x9003e,0x9003f,0x90040,0x90041,
  0x90042,0x90043,0x90044,0x90045,0x90046,0x90047,0x90048,0x90049,0x9004a,0x9004b,0x9004c,0x9004d,0x9004e,0x9004f,0x90050,0x90051,
  0x90052,0x90053,0x90054,0x90055,0x90056,0x90057,0x90058,0x90059,0x9005a,0x9005b,0x9005c,0x9005d,0x9005e,0x9005f,0x90060,0x90061,
  0x90062,0x90063,0x00100,0x00101,0x00102,0x00103,0x00104,0x00105,0x00106,0x00107,0x00108,0x00109,0x0010a,0x0010b,0x0010c,0x0010d,
  0x0010e,0x0010f,0x00110,0x00111,0x00112,0x00113,0x00114,0x00115,0x00116,0x00117,0x00118,0x00119,0x0011a,0x0011b,0x0011c,0x0011d,
  0x0011e,0x0011f,0x00120,0x00121,0x00122,0x00123,0x00124,0x00125,0x00126,0x00127,0x00128,0x00129,0x0012a,0x0012b,0x0012c,0x0012d,
  0x0012e,0x0012f,0x00130,0x00131,0x00132,0x00133,0x00134,0x00135,0x00136,0x00137,0x00138,0x00139,0x0013a,0x0013b,0x0013c,0x0013d,
  0x0013e,0x0013f,0x02603,0x02604,0x02605,0x02606,0x02607,0x02608,0x02609,0x0260a,0x0260b,0x0260c,0x0260d,0x0260e,0x0260f,0x02610,
  0x02611,0x02612,0x02613,0x91000,0x91001,0x91002,0x91003,0x91004,0x91005,0x91006,0x91007,0x91008,0x02400,0x02401,0x02402,0x02403,
  0x02404,0x02405,0x02406,0x02407,0x02408,0x02409,0x0240a,0x0240b,0x0240c,0x0240d,0x0240e,0x0240f,0x02410,0x02411,0x02412,0x02413,
  0x02414,0x02415,0x02416,0x02417,0x02418,0x02419,0x0220b,0x0220c,0x0220d,0x0220e,0x0220f,0x02210,0x02211,0x02212,0x02213,0x02214,
  0x02215,0x02216,0x02217,0x80100,0x80101,0x80102,0x80103,0x80104,0x80105,0x80106,0x80107,0x80108,0x80109,0x8010a,0x8010b,0x8010c,
  0x8010d,0x8010e,0x8010f,0x80110,0x80111,0x80112,0x80113,0x80114,0x80115,0x80116,0x80117,0x02c00,0x02c01,0x02c02,0x02c03,0x02c04,
  0x02c05,0x80000,0x80001,0x80002,0x80003,0x80004,0x80005,0x80006,0x80007,0x80008,0x80009,0x8000a,0x8000b,0x8000c,0x8000d,0x8000e,
  0x8000f,0x80010,0x80011,0x80012,0x80013,0x80014,0x80015,0x80016,0x80017,0x80018,0x80019,0x8001a,0x8001b,0x8001c,0x8001d,0x8001e,
  0x8001f,0x02218,0x02219,0x0221a,0x0221b,0x0221c,0x0221d,0x0221e,0x0221f,0x91009,0x9100a,0x9100b,0x9100c,0x9100d,0x9100e,0x9100f,
  0x91010,0x91011,0x91012,0x91013,0x91014,0x91015,0x91016,0x91017,0x91018,0x91019,0x9101a,0x9101b,0x9101c,0x9101d,0x9101e,0x9101f,
  0x91020,0x02800,0x02801,0x02802,0x82200,0x82201,0x82202,0x82203,0x82204,0x82205,0x82206,0x82207,0x82208,0x82209,0x8220a,0x8220b,
  0x8220c,0x8220d,0x90064,0x90065,0x90066,0x90067,0x90068,0x90069,0x02803,0x82800,0x82801,0x82802,0x82803,0x02614,0x10000,0x10001,
  0x10002,0x10003,0x10004,0x10005,0x10006,0x10007,0x10008,0x10009,0x1000a,0x1000b,0x1000c,0x1000d,0x1000e,0x1000f,0x80118,0x82804,
  0x91021,0x91022,0x91023,0x91024,0x91025,0x91026,0x91027,0x91028,0x91029,0x9102a,0x9102b,0x9102c,0x9102d,0x9102e,0x9102f,0x91030,
  0x91031,0x91032,0x91033,0x91034,0x91035,0x91036,0x91037,0x91038,0x91039,0x9103a,0x9103b,0x9103c,0x9103d,0x9103e,0x9103f,0x91040,
  0x91041,0x91042,0x91043,0x91044,0x91045,0x91046,0x91047,0x91048,0x91049,0x9104a,0x9104b,0x9104c,0x9104d,0x9104e,0x9104f,0x91050,
  0x91051,0x91052,0x91053,0x91054,0x91055,0x91056,0x91057,0x91058,0x91059,0x9105a,0x9105b,0x9105c,0x9105d,0x9105e,0x9105f,0x91060,
  0x91061,0x91062,0x91063,0x91064,0x91065,0x91066,0x91067,0x91068,0x91069,0x9106a,0x9106b,0x9106c,0x9106d,0x82400,0x82401,0x82402,
  0x82403,0x82404,0x82405,0x82406,0x82407,0x82408,0x82409,0x8240a,0x8240b,0x8240c,0x8240d,0x8240e,0x8240f,0x80119,0x8011a,0x20000,
  0x02615,0x02616,0x02617,0x02618,0x02619,0x0261a,0x0261b,0x82805,0x82806,0x10100,0x10101,0x0241a,0x9106e,0x9106f,0x30000,0x30001,
  0x30002,0x8220e,0x8220f,0x20001,0x20002,0x20003,0x20004,0x20005,0x9006a,0x9006b,0x9006c,0x9006d,0x9006e,0x9006f,0x8011b,0x8011c,
  0x8011d,0x8011e,0x8011f,0x02a00,0x20006,0x20007,0x20008,0x20009,0x2000a,0x2000b,0x2000c,0x2000d,0x2000e,0x2000f,0x20010,0x20011,
  0x20012,0x20013,0x20014,0x20015,0x20016,0x20017,0x20018,0x20019,0x2001a,0x2001b,0x2001c,0x2001d,0x2001e,0x2001f,0x82600,0x82601,
  0x82602,0x82603,0x82604,0x82605,0x82606,0x82607,0x82608,0x82609,0x8260a,0x8260b,0x8260c,0x8260d,0x8260e,0x8260f,0x02a01,0x02a02,
  0x02804,0x02805,0x02806,0x02807,0x02808,0x02809,0x0280a,0x0280b,0x0280c,0x0280d,0x0280e,0x0280f,0x02810,0x02811,0x02812,0x02813,
  0x02814,0x02815,0x02816,0x02817,0x02818,0x02819,0x0281a,0x0281b,0x0281c,0x0281d,0x0281e,0x0281f,0x0261c,0x0261d,0x02c06,0x02c07,
  0x40000,0x0241b,0x0241c,0x0241d,0x0241e,0x0241f,0x30003,0x30004,0x30005,0x30006,0x30007,0x30008,0x30009,0x3000a,0x3000b,0x3000c,
  0x3000d,0x3000e,0x3000f,0x82a00,0x02a03,0x02a04,0x02a05,0x10102,0x10103,0x10104,0x10105,0x10106,0x10107,0x10108,0x10109,0x1010a,
  0x1010b,0x1010c,0x1010d,0x1010e,0x1010f,0x20100,0x20101,0x20102,0x20103,0x20104,0x20105,0x20106,0x20107,0x20108,0x20109,0x2010a,
  0x2010b,0x2010c,0x2010d,0x2010e,0x2010f,0x20110,0x20111,0x20112,0x20113,0x20114,0x20115,0x20116,0x20117,0x20118,0x20119,0x2011a,
  0x2011b,0x2011c,0x2011d,0x2011e,0x2011f,0x40001,0x40002,0x40003,0x40004,0x40005,0x40006,0x40007,0x40008,0x40009,0x4000a,0x4000b,
  0x4000c,0x4000d,0x4000e,0x4000f,0x40010,0x40011,0x40012,0x40013,0x40014,0x40015,0x40016,0x40017,0x40018,0x40019,0x4001a,0x4001b,
  0x02a06,0x02a07,0x02a08,0x02a09,0x02a0a,0x02a0b,0x02a0c,0x02a0d,0x02a0e,0x02a0f,0x02a10,0x30100,0x30101,0x30102,0x30103,0x30104,
  0x30105,0x30106,0x30107,0x30108,0x82a01,0x82a02,0x82a03,0x82a04,0x82a05,0x82a06,0x82a07,0x82a08,0x82a09,0x82a0a,0x02a11,0x02a12,
  0x02a13,0x02a14,0x02a15,0x02a16,0x02a17,0x02a18,0x02a19,0x02a1a,0x02a1b,0x02a1c,0x02a1d,0x02a1e,0x02a1f,0x83000,0x30109,0x3010a,
  0x3010b,0x3010c,0x3010d,0x3010e,0x3010f,0x40100,0x40101,0x40102,0x40103,0x40104,0x40105,0x40106,0x40107,0x40108,0x40109,0x4010a,
  0x4010b,0x4010c,0x4010d,0x4010e,0x4010f,0x40110,0x40111,0x40112,0x40113,0x40114,0x40115,0x40116,0x40117,0x40118,0x40119,0x4011a,
  0x4011b,0x82c00,0x82c01,0x82807,0x82808,0x82809,0x8280a,0x8280b,0x8280c,0x8280d,0x82a0b,0x82a0c,0x50000,0x50001,0x50002,0x50003,
  0x50004,0x50005,0x50006,0x50007,0x50008,0x50009,0x5000a,0x5000b,0x5000c,0x5000d,0x5000e,0x5000f,0x50010,0x50011,0x50012,0x50013,
  0x50014,0x50015,0x50016,0x50017,0x50018,0x50019,0x5001a,0x5001b,0x5001c,0x5001d,0x5001e,0x5001f,0x10200,0x10201,0x10202,0x10203,
  0x10204,0x10205,0x10206,0x10207,0x10208,0x10209,0x1020a,0x1020b,0x1020c,0x1020d,0x1020e,0x1020f,0x20200,0x20201,0x20202,0x20203,
  0x20204,0x20205,0x20206,0x20207,0x20208,0x20209,0x2020a,0x2020b,0x2020c,0x2020d,0x2020e,0x2020f,0x20210,0x20211,0x20212,0x20213,
  0x20214,0x20215,0x20216,0x20217,0x20218,0x20219,0x2021a,0x2021b,0x2021c,0x2021d,0x2021e,0x2021f,0x8280e,0x8280f,0x0261e,0x0261f,
  0x82a0d,0x82a0e,0x82a0f,0x60000,0x60001,0x60002,0x60003,0x60004,0x60005,0x60006,0x60007,0x60008,0x60009,0x6000a,0x6000b,0x6000c,
  0x6000d,0x6000e,0x6000f,0x50100,0x50101,0x50102,0x50103,0x50104,0x50105,0x50106,0x50107,0x50108,0x50109,0x5010a,0x5010b,0x5010c,
  0x5010d,0x5010e,0x5010f,0x50110,0x50111,0x50112,0x50113,0x50114,0x50115,0x50116,0x50117,0x50118,0x50119,0x5011a,0x5011b,0x5011c,
  0x5011d,0x5011e,0x5011f,0x01000,0x01001,0x01002,0x01003,0x01004,0x01005,0x01006,0x01007,0x01008,0x01009,0x0100a,0x0100b,0x0100c,
  0x0100d,0x0100e,0x0100f,0x01010,0x01011,0x01012,0x01013,0x01014,0x01015,0x01016,0x01017,0x01018,0x01019,0x0101a,0x0101b,0x0101c,
  0x0101d,0x0101e,0x0101f,0x01020,0x01021,0x01022,0x01023,0x01024,0x01025,0x01026,0x01027,0x01028,0x02c08,0x02c09,0x30200,0x30201,
  0x30202,0x30203,0x30204,0x30205,0x30206,0x30207,0x30208,0x30209,0x3020a,0x3020b,0x3020c,0x3020d,0x3020e,0x3020f,0x10300,0x10301,
  0x10302,0x10303,0x10304,0x10305,0x10306,0x10307,0x10308,0x10309,0x1030a,0x1030b,0x1030c,0x1030d,0x1030e,0x1030f,0x01029,0x0102a,
  0x0102b,0x0102c,0x0102d,0x0102e,0x0102f,0x01030,0x01031,0x01032,0x01033,0x01034,0x01035,0x01036,0x01037,0x01038,0x01039,0x0103a,
  0x0103b,0x0103c,0x0103d,0x0103e,0x0103f,0x82e00,0x40200,0x40201,0x40202,0x40203,0x40204,0x40205,0x40206,0x40207,0x40208,0x40209,
  0x4020a,0x4020b,0x4020c,0x4020d,0x4020e,0x4020f,0x40210,0x40211,0x40212,0x40213,0x40214,0x40215,0x40216,0x40217,0x40218,0x40219,
  0x4021a,0x4021b,0x60100,0x60101,0x60102,0x60103,0x60104,0x60105,0x60106,0x60107,0x60108,0x60109,0x6010a,0x6010b,0x6010c,0x6010d,
  0x6010e,0x6010f,0x20300,0x20301,0x20302,0x20303,0x20304,0x20305,0x20306,0x20307,0x20308,0x20309,0x2030a,0x2030b,0x2030c,0x2030d,
  0x2030e,0x2030f,0x20310,0x20311,0x20312,0x20313,0x20314,0x20315,0x20316,0x20317,0x20318,0x20319,0x2031a,0x2031b,0x2031c,0x2031d,
  0x2031e,0x2031f,0x02e00,0x02e01,0x02e02,0x82c02,0x82c03,0x82c04,0x82c05,0x82c06,0x82c07,0x82c08,0x02e03,0x02e04,0x02e05,0x02e06,
  0x01100,0x01101,0x01102,0x01103,0x01104,0x01105,0x01106,0x01107,0x01108,0x01109,0x0110a,0x0110b,0x0110c,0x0110d,0x0110e,0x0110f,
  0x01110,0x01111,0x01112,0x01113,0x01114,0x01115,0x01116,0x01117,0x01118,0x01119,0x0111a,0x0111b,0x0111c,0x0111d,0x0111e,0x0111f,
  0x01120,0x01121,0x01122,0x01123,0x01124,0x01125,0x01126,0x01127,0x01128,0x01129,0x0112a,0x0112b,0x0112c,0x0112d,0x0112e,0x0112f,
  0x01130,0x01131,0x01132,0x01133,0x01134,0x01135,0x01136,0x01137,0x01138,0x01139,0x0113a,0x0113b,0x0113c,0x0113d,0x0113e,0x0113f,
  0x82e01,0x82e02,0x82e03,0x82e04,0x82e05,0x82e06,0x82e07,0x02e07,0x02e08,0x02e09,0x02e0a,0x02e0b,0x02e0c,0x02e0d,0x02e0e,0x02e0f,
  0x02e10,0x02e11,0x02e12,0x30400,0x02e13,0x60200,0x10500,0x10501,0x10502,0x10503,0x10504,0x10505,0x30300,0x30301,0x30302,0x30303,
  0x30304,0x30305,0x30306,0x30307,0x30308,0x30309,0x3030a,0x3030b,0x3030c,0x3030d,0x3030e,0x3030f,0x50200,0x50201,0x50202,0x50203,
  0x50204,0x50205,0x50206,0x50207,0x50208,0x50209,0x5020a,0x5020b,0x5020c,0x5020d,0x5020e,0x5020f,0x50210,0x50211,0x50212,0x50213,
  0x50214,0x50215,0x50216,0x50217,0x50218,0x50219,0x5021a,0x5021b,0x5021c,0x5021d,0x5021e,0x5021f,0x02c0a,0x02c0b,0x02c0c,0x02c0d,
  0x02c0e,0x02c0f,0x02c10,0x40300,0x40301,0x40302,0x40303,0x40304,0x40305,0x40306,0x40307,0x40308,0x40309,0x4030a,0x4030b,0x4030c,
  0x4030d,0x4030e,0x4030f,0x40310,0x40311,0x40312,0x40313,0x40314,0x40315,0x40316,0x40317,0x40318,0x40319,0x4031a,0x4031b,0x82c09,
  0x82c0a,0x10400,0x10401,0x10402,0x10403,0x10404,0x10405,0x10406,0x10407,0x10408,0x10409,0x1040a,0x1040b,0x1040c,0x1040d,0x1040e,
  0x1040f,0x50300,0x50301,0x02c11,0x02c12,0x02c13,0x02c14,0x02c15,0x02c16,0x02c17,0x02c18,0x02c19,0x02c1a,0x02c1b,0x02c1c,0x02c1d,
  0x02c1e,0x02c1f,0x02e14,0x02e15,0x02e16,0x02e17,0x02e18,0x02e19,0x20400,0x20401,0x20402,0x20403,0x20404,0x20405,0x20406,0x60201,
  0x60202,0x60203,0x60204,0x60205,0x60206,0x60207,0x60208,0x60209,0x6020a,0x6020b,0x6020c,0x6020d,0x6020e,0x6020f,0x20407,0x20408,
  0x20409,0x2040a,0x2040b,0x2040c,0x2040d,0x2040e,0x2040f,0x20410,0x20411,0x20412,0x20413,0x20414,0x20415,0x20416,0x20417,0x20418,
  0x20419,0x2041a,0x2041b,0x2041c,0x2041d,0x2041e,0x2041f,0x10506,0x10507,0x10508,0x10509,0x1050a,0x1050b,0x50302,0x50303,0x50304,
  0x50305,0x50306,0x50307,0x50308,0x50309,0x5030a,0x5030b,0x5030c,0x5030d,0x5030e,0x5030f,0x50310,0x50311,0x50312,0x50313,0x50314,
  0x50315,0x50316,0x50317,0x50318,0x50319,0x5031a,0x5031b,0x5031c,0x5031d,0x5031e,0x5031f,0x82c0b,0x82c0c,0x82c0d,0x01200,0x01201,
  0x01202,0x01203,0x01204,0x01205,0x01206,0x01207,0x01208,0x01209,0x0120a,0x0120b,0x0120c,0x0120d,0x0120e,0x0120f,0x01210,0x01211,
  0x01212,0x01213,0x01214,0x01215,0x01216,0x01217,0x01218,0x01219,0x0121a,0x0121b,0x0121c,0x0121d,0x0121e,0x0121f,0x01220,0x01221,
  0x01222,0x01223,0x01224,0x01225,0x01226,0x01227,0x01228,0x01229,0x0122a,0x0122b,0x0122c,0x0122d,0x0122e,0x0122f,0x01230,0x01231,
  0x01232,0x01233,0x01234,0x01235,0x01236,0x01237,0x01238,0x01239,0x0123a,0x0123b,0x0123c,0x0123d,0x0123e,0x0123f,0x30401,0x30402,
  0x30403,0x30404,0x30405,0x30406,0x30407,0x30408,0x30409,0x3040a,0x3040b,0x3040c,0x3040d,0x3040e,0x3040f,0x1050c,0x1050d,0x1050e,
  0x1050f,0x11100,0x60300,0x60301,0x60302,0x60303,0x60304,0x60305,0x60306,0x60307,0x60308,0x60309,0x6030a,0x6030b,0x6030c,0x6030d,
  0x6030e,0x6030f,0x83001,0x83002,0x83003,0x83004,0x83005,0x40400,0x82c0e,0x82c0f,0x40401,0x40402,0x40403,0x40404,0x40405,0x40406,
  0x40407,0x40408,0x40409,0x4040a,0x4040b,0x4040c,0x4040d,0x4040e,0x4040f,0x40410,0x40411,0x40412,0x40413,0x40414,0x40415,0x40416,
  0x40417,0x40418,0x40419,0x4041a,0x4041b,0x82e08,0x82e09,0x82e0a,0x82e0b,0x83006,0x83007,0x83008,0x83009,0x8300a,0x8300b,0x8300c,
  0x8300d,0x8300e,0x8300f,0x83200,0x83201,0x83202,0x83203,0x83204,0x83205,0x83206,0x01300,0x01301,0x01302,0x01303,0x01304,0x01305,
  0x01306,0x01307,0x01308,0x01309,0x0130a,0x0130b,0x0130c,0x0130d,0x0130e,0x0130f,0x01310,0x01311,0x01312,0x01313,0x20500,0x20501,
  0x20502,0x20503,0x20504,0x20505,0x20506,0x20507,0x20508,0x20509,0x2050a,0x2050b,0x2050c,0x2050d,0x2050e,0x2050f,0x20510,0x20511,
  0x20512,0x20513,0x20514,0x20515,0x20516,0x20517,0x20518,0x20519,0x2051a,0x01314,0x01315,0x01316,0x01317,0x01318,0x01319,0x0131a,
  0x0131b,0x0131c,0x0131d,0x0131e,0x0131f,0x01320,0x01321,0x01322,0x01323,0x2051b,0x2051c,0x2051d,0x2051e,0x2051f,0x82e0c,0x01324,
  0x01325,0x01326,0x01327,0x01328,0x01329,0x0132a,0x0132b,0x0132c,0x0132d,0x0132e,0x0132f,0x01330,0x01331,0x01332,0x01333,0x01334,
  0x01335,0x01336,0x01337,0x01338,0x01339,0x0133a,0x0133b,0x0133c,0x0133d,0x0133e,0x0133f,0x10600,0x10601,0x10602,0x10603,0x10604,
  0x10605,0x10606,0x10607,0x10608,0x10609,0x1060a,0x1060b,0x1060c,0x1060d,0x1060e,0x1060f,0x83207,0x50400,0x50401,0x50402,0x50403,
  0x50404,0x50405,0x50406,0x50407,0x50408,0x50409,0x5040a,0x5040b,0x5040c,0x5040d,0x5040e,0x5040f,0x50410,0x50411,0x50412,0x50413,
  0x50414,0x50415,0x50416,0x50417,0x50418,0x50419,0x5041a,0x5041b,0x5041c,0x5041d,0x5041e,0x30500,0x30501,0x30502,0x30503,0x30504,
  0x30505,0x30506,0x30507,0x30508,0x30509,0x3050a,0x3050b,0x3050c,0x3050d,0x3050e,0x3050f,0x82e0d,0x82e0e,0x82e0f,0x5041f,0x20600,
  0x20601,0x20602,0x20603,0x20604,0x20605,0x20606,0x20607,0x20608,0x20609,0x2060a,0x2060b,0x2060c,0x2060d,0x2060e,0x2060f,0x20610,
  0x20611,0x20612,0x20613,0x20614,0x20615,0x20616,0x20617,0x20618,0x20619,0x2061a,0x2061b,0x2061c,0x2061d,0x2061e,0x2061f,0x83208,
  0x83209,0x8320a,0x8320b,0x01400,0x01401,0x10700,0x10701,0x10702,0x10703,0x40500,0x40501,0x40502,0x40503,0x40504,0x40505,0x40506,
  0x40507,0x40508,0x40509,0x4050a,0x4050b,0x4050c,0x4050d,0x4050e,0x4050f,0x40510,0x40511,0x40512,0x40513,0x40514,0x40515,0x40516,
  0x40517,0x40518,0x40519,0x4051a,0x4051b,0x60400,0x60401,0x60402,0x60403,0x60404,0x60405,0x60406,0x60407,0x60408,0x60409,0x6040a,
  0x6040b,0x6040c,0x6040d,0x6040e,0x6040f,0x8320c,0x8320d,0x8320e,0x8320f,0x01402,0x01403,0x01404,0x01405,0x01406,0x01407,0x01408,
  0x01409,0x0140a,0x0140b,0x0140c,0x0140d,0x0140e,0x0140f,0x01410,0x01411,0x01412,0x01413,0x01414,0x01415,0x01416,0x01417,0x01418,
  0x01419,0x0141a,0x0141b,0x0141c,0x0141d,0x0141e,0x0141f,0x01420,0x01421,0x01422,0x01423,0x01424,0x01425,0x01426,0x01427,0x01428,
  0x01429,0x0142a,0x0142b,0x0142c,0x0142d,0x0142e,0x0142f,0x01430,0x01431,0x01432,0x01433,0x01434,0x01435,0x01436,0x01437,0x01438,
  0x01439,0x0143a,0x0143b,0x0143c,0x0143d,0x0143e,0x0143f,0x11000,0x11001,0x10704,0x10705,0x10706,0x10707,0x10708,0x10709,0x1070a,
  0x1070b,0x1070c,0x1070d,0x1070e,0x1070f,0x50500,0x50501,0x50502,0x50503,0x50504,0x50505,0x50506,0x50507,0x50508,0x50509,0x5050a,
  0x5050b,0x5050c,0x5050d,0x5050e,0x5050f,0x50510,0x50511,0x50512,0x50513,0x50514,0x50515,0x50516,0x50517,0x50518,0x50519,0x5051a,
  0x5051b,0x5051c,0x5051d,0x5051e,0x5051f,0x11200,0x30600,0x30601,0x30602,0x30603,0x30604,0x30605,0x30606,0x30607,0x30608,0x30609,
  0x3060a,0x3060b,0x3060c,0x3060d,0x3060e,0x3060f,0x01600,0x60500,0x60501,0x60502,0x60503,0x60504,0x60505,0x60506,0x60507,0x60508,
  0x60509,0x6050a,0x6050b,0x6050c,0x02e1a,0x02e1b,0x02e1c,0x50600,0x50601,0x50602,0x01500,0x01501,0x01502,0x01503,0x20700,0x20701,
  0x20702,0x20703,0x20704,0x20705,0x20706,0x20707,0x20708,0x20709,0x2070a,0x2070b,0x2070c,0x2070d,0x2070e,0x2070f,0x20710,0x20711,
  0x20712,0x20713,0x20714,0x20715,0x20716,0x20717,0x20718,0x20719,0x2071a,0x2071b,0x30700,0x30701,0x30702,0x30703,0x30704,0x30705,
  0x30706,0x30707,0x30708,0x30709,0x3070a,0x3070b,0x3070c,0x3070d,0x3070e,0x3070f,0x2071c,0x2071d,0x2071e,0x2071f,0x6050d,0x6050e,
  0x6050f,0x11101,0x11102,0x11103,0x11104,0x11105,0x11106,0x11107,0x11108,0x11109,0x1110a,0x1110b,0x1110c,0x1110d,0x1110e,0x1110f,
  0x40600,0x40601,0x40602,0x40603,0x40604,0x40605,0x40606,0x40607,0x40608,0x40609,0x4060a,0x4060b,0x4060c,0x4060d,0x4060e,0x4060f,
  0x40610,0x40611,0x40612,0x40613,0x40614,0x40615,0x40616,0x40617,0x40618,0x40619,0x4061a,0x4061b,0x01504,0x01505,0x01506,0x01507,
  0x01508,0x01509,0x0150a,0x0150b,0x0150c,0x0150d,0x0150e,0x0150f,0x01510,0x01511,0x01512,0x01513,0x01514,0x01515,0x01516,0x01517,
  0x01518,0x01519,0x0151a,0x0151b,0x0151c,0x0151d,0x0151e,0x0151f,0x01520,0x01521,0x01522,0x01523,0x01524,0x01525,0x01526,0x01527,
  0x01528,0x01529,0x0152a,0x0152b,0x0152c,0x0152d,0x0152e,0x0152f,0x01530,0x01531,0x01532,0x01533,0x01534,0x01535,0x01536,0x01537,
  0x01538,0x01539,0x0153a,0x0153b,0x0153c,0x0153d,0x0153e,0x0153f,0x11201,0x11202,0x11203,0x11204,0x11205,0x11206,0x11207,0x11208,
  0x11209,0x1120a,0x1120b,0x83400,0x83401,0x10800,0x10801,0x10802,0x10803,0x10804,0x10805,0x10806,0x10807,0x10808,0x10809,0x1080a,
  0x1080b,0x1080c,0x1080d,0x1080e,0x1080f,0x60600,0x02e1d,0x02e1e,0x02e1f,0x83402,0x83403,0x83404,0x83405,0x83406,0x83407,0x83408,
  0x50700,0x50701,0x40700,0x40701,0x40702,0x40703,0x40704,0x40705,0x40706,0x40707,0x40708,0x40709,0x4070a,0x4070b,0x4070c,0x4070d,
  0x4070e,0x4070f,0x40710,0x40711,0x40712,0x40713,0x40714,0x40715,0x40716,0x40717,0x40718,0x40719,0x4071a,0x4071b,0x20800,0x20801,
  0x20802,0x20803,0x20804,0x20805,0x20806,0x20807,0x83409,0x8340a,0x8340b,0x8340c,0x8340d,0x8340e,0x50603,0x50604,0x50605,0x50606,
  0x50607,0x50608,0x50609,0x5060a,0x5060b,0x5060c,0x5060d,0x5060e,0x5060f,0x50610,0x50611,0x50612,0x50613,0x50614,0x50615,0x50616,
  0x50617,0x50618,0x50619,0x5061a,0x5061b,0x5061c,0x5061d,0x20808,0x20809,0x2080a,0x2080b,0x2080c,0x2080d,0x2080e,0x2080f,0x27000,
  0x27001,0x5061e,0x5061f,0x20810,0x20811,0x20812,0x20813,0x20814,0x20815,0x20816,0x20817,0x20818,0x20819,0x2081a,0x2081b,0x2081c,
  0x2081d,0x2081e,0x2081f,0x11002,0x11003,0x11004,0x11005,0x11006,0x11007,0x11008,0x11009,0x1100a,0x1100b,0x1100c,0x1100d,0x1100e,
  0x1100f,0x1120c,0x1120d,0x1120e,0x1120f,0x60601,0x60602,0x60603,0x60604,0x60605,0x60606,0x60607,0x60608,0x60609,0x6060a,0x6060b,
  0x6060c,0x6060d,0x8340f,0x6060e,0x6060f,0x11300,0x11301,0x11302,0x11303,0x11304,0x11305,0x11306,0x11307,0x11308,0x11309,0x1130a,
  0x1130b,0x1130c,0x1130d,0x1130e,0x1130f,0x50702,0x50703,0x50704,0x50705,0x50706,0x50707,0x50708,0x50709,0x5070a,0x5070b,0x5070c,
  0x5070d,0x5070e,0x5070f,0x50710,0x50711,0x50712,0x50713,0x50714,0x50715,0x50716,0x50717,0x50718,0x50719,0x5071a,0x5071b,0x5071c,
  0x5071d,0x5071e,0x5071f,0x10900,0x30800,0x30801,0x30802,0x30803,0x30804,0x30805,0x30806,0x30807,0x30808,0x30809,0x3080a,0x3080b,
  0x3080c,0x3080d,0x3080e,0x3080f,0x27002,0x27003,0x27004,0x27005,0x27006,0x27007,0x01601,0x01602,0x01603,0x01604,0x01605,0x01606,
  0x01607,0x01608,0x01609,0x0160a,0x0160b,0x0160c,0x0160d,0x0160e,0x0160f,0x01610,0x01611,0x01612,0x01613,0x01614,0x01615,0x01616,
  0x01617,0x01618,0x01619,0x0161a,0x0161b,0x0161c,0x0161d,0x0161e,0x0161f,0x01620,0x01621,0x01622,0x01623,0x01624,0x01625,0x01626,
  0x01627,0x01628,0x01629,0x0162a,0x0162b,0x0162c,0x0162d,0x0162e,0x0162f,0x01630,0x01631,0x01632,0x01633,0x01634,0x01635,0x01636,
  0x01637,0x01638,0x01639,0x0163a,0x0163b,0x0163c,0x0163d,0x0163e,0x0163f,0x10901,0x10902,0x10903,0x10904,0x10905,0x10906,0x10907,
  0x10908,0x10909,0x1090a,0x1090b,0x1090c,0x1090d,0x1090e,0x1090f,0x83600,0x83601,0x60700,0x60701,0x60702,0x60703,0x60704,0x60705,
  0x60706,0x60707,0x60708,0x60709,0x6070a,0x6070b,0x6070c,0x6070d,0x6070e,0x6070f,0x40800,0x40801,0x40802,0x40803,0x40804,0x40805,
  0x40806,0x40807,0x40808,0x40809,0x4080a,0x4080b,0x4080c,0x4080d,0x4080e,0x4080f,0x40810,0x40811,0x40812,0x40813,0x40814,0x40815,
  0x40816,0x40817,0x40818,0x40819,0x4081a,0x4081b,0x01700,0x01701,0x01702,0x01703,0x01704,0x01705,0x01706,0x01707,0x01708,0x01709,
  0x0170a,0x0170b,0x0170c,0x0170d,0x0170e,0x0170f,0x01710,0x01711,0x01712,0x01713,0x01714,0x01715,0x01716,0x01717,0x01718,0x01719,
  0x0171a,0x0171b,0x0171c,0x0171d,0x0171e,0x0171f,0x01720,0x01721,0x01722,0x01723,0x01724,0x01725,0x01726,0x01727,0x01728,0x01729,
  0x0172a,0x0172b,0x0172c,0x0172d,0x0172e,0x0172f,0x01730,0x01731,0x01732,0x01733,0x01734,0x01735,0x01736,0x01737,0x01738,0x01739,
  0x0173a,0x0173b,0x0173c,0x0173d,0x0173e,0x0173f,0x20900,0x20901,0x20902,0x20903,0x20904,0x20905,0x20906,0x20907,0x20908,0x20909,
  0x2090a,0x2090b,0x2090c,0x2090d,0x2090e,0x2090f,0x20910,0x20911,0x20912,0x20913,0x20914,0x20915,0x20916,0x20917,0x20918,0x20919,
  0x2091a,0x2091b,0x2091c,0x2091d,0x2091e,0x2091f,0x11500,0x11501,0x11502,0x11503,0x27200,0x27201,0x27202,0x40a00,0x40a01,0x11504,
  0x11505,0x11506,0x11507,0x11508,0x11509,0x1150a,0x50800,0x27400,0x27401,0x27402,0x27403,0x27404,0x27405,0x27203,0x27204,0x27205,
  0x1150b,0x1150c,0x27406,0x11400,0x11401,0x11402,0x11403,0x11404,0x11405,0x10a00,0x10a01,0x10a02,0x10a03,0x10a04,0x10a05,0x10a06,
  0x10a07,0x10a08,0x10a09,0x10a0a,0x10a0b,0x10a0c,0x10a0d,0x10a0e,0x10a0f,0x01800,0x01801,0x50801,0x50802,0x50803,0x50804,0x50805,
  0x50806,0x50807,0x50808,0x50809,0x5080a,0x5080b,0x5080c,0x5080d,0x5080e,0x5080f,0x50810,0x50811,0x50812,0x50813,0x50814,0x50815,
  0x50816,0x50817,0x50818,0x50819,0x5081a,0x5081b,0x5081c,0x5081d,0x5081e,0x5081f,0x11406,0x11407,0x11408,0x11409,0x1140a,0x1140b,
  0x1140c,0x1140d,0x1140e,0x1140f,0x1150d,0x1150e,0x1150f,0x30a00,0x27407,0x83602,0x83603,0x83604,0x83605,0x83606,0x83607,0x83608,
  0x83609,0x27206,0x27207,0x30900,0x30901,0x30902,0x30903,0x30904,0x30905,0x30906,0x30907,0x30908,0x30909,0x3090a,0x3090b,0x3090c,
  0x3090d,0x3090e,0x3090f,0x20a00,0x20a01,0x20a02,0x20a03,0x20a04,0x20a05,0x20a06,0x20a07,0x20a08,0x20a09,0x20a0a,0x20a0b,0x20a0c,
  0x20a0d,0x20a0e,0x20a0f,0x20a10,0x20a11,0x20a12,0x20a13,0x20a14,0x60800,0x60801,0x20a15,0x20a16,0x20a17,0x20a18,0x20a19,0x20a1a,
  0x20a1b,0x20a1c,0x20a1d,0x20a1e,0x20a1f,0x35000,0x35001,0x60802,0x60803,0x60804,0x60805,0x60806,0x60807,0x60808,0x60809,0x6080a,
  0x6080b,0x6080c,0x8360a,0x8360b,0x8360c,0x8360d,0x8360e,0x8360f,0x40900,0x40901,0x40902,0x40903,0x40904,0x40905,0x40906,0x40907,
  0x40908,0x40909,0x4090a,0x4090b,0x4090c,0x4090d,0x4090e,0x4090f,0x40910,0x40911,0x40912,0x40913,0x40914,0x40915,0x40916,0x40917,
  0x40918,0x40919,0x4091a,0x4091b,0x6080d,0x6080e,0x6080f,0x30a01,0x30a02,0x30a03,0x30a04,0x30a05,0x30a06,0x30a07,0x30a08,0x30a09,
  0x30a0a,0x30a0b,0x30a0c,0x30a0d,0x30a0e,0x30a0f,0x01802,0x01803,0x01804,0x01805,0x01806,0x01807,0x01808,0x01809,0x0180a,0x0180b,
  0x0180c,0x0180d,0x0180e,0x0180f,0x01810,0x01811,0x01812,0x01813,0x01814,0x01815,0x01816,0x01817,0x01818,0x01819,0x0181a,0x0181b,
  0x0181c,0x0181d,0x0181e,0x0181f,0x01820,0x01821,0x01822,0x01823,0x01824,0x01825,0x01826,0x01827,0x01828,0x01829,0x0182a,0x0182b,
  0x0182c,0x0182d,0x0182e,0x0182f,0x01830,0x01831,0x01832,0x01833,0x01834,0x01835,0x01836,0x01837,0x01838,0x01839,0x0183a,0x0183b,
  0x0183c,0x0183d,0x0183e,0x0183f,0x50900,0x50901,0x40a02,0x40a03,0x40a04,0x40a05,0x40a06,0x40a07,0x40a08,0x40a09,0x40a0a,0x40a0b,
  0x40a0c,0x40a0d,0x40a0e,0x40a0f,0x40a10,0x40a11,0x40a12,0x40a13,0x40a14,0x40a15,0x40a16,0x40a17,0x40a18,0x40a19,0x40a1a,0x40a1b,
  0x50902,0x50903,0x50904,0x50905,0x50906,0x50907,0x50908,0x50909,0x5090a,0x5090b,0x5090c,0x5090d,0x5090e,0x5090f,0x50910,0x50911,
  0x50912,0x50913,0x50914,0x50915,0x50916,0x50917,0x50918,0x50919,0x5091a,0x5091b,0x5091c,0x5091d,0x5091e,0x5091f,0x10b00,0x10b01,
  0x10b02,0x10b03,0x10b04,0x10b05,0x10b06,0x10b07,0x10b08,0x10b09,0x10b0a,0x10b0b,0x10b0c,0x10b0d,0x10b0e,0x10b0f,0x10c00,0x10c01,
  0x10c02,0x10c03,0x10c04,0x10c05,0x10c06,0x10c07,0x10c08,0x10c09,0x10c0a,0x10c0b,0x11600,0x11601,0x11602,0x11603,0x11604,0x11605,
  0x11606,0x11607,0x11608,0x11609,0x60900,0x60901,0x60902,0x60903,0x60904,0x60905,0x60906,0x60907,0x60908,0x60909,0x6090a,0x6090b,
  0x6090c,0x6090d,0x6090e,0x6090f,0x20b00,0x20b01,0x20b02,0x20b03,0x20b04,0x20b05,0x20b06,0x20b07,0x20b08,0x20b09,0x20b0a,0x20b0b,
  0x20b0c,0x20b0d,0x20b0e,0x20b0f,0x20b10,0x20b11,0x20b12,0x20b13,0x20b14,0x20b15,0x20b16,0x20b17,0x20b18,0x20b19,0x20b1a,0x20b1b,
  0x20b1c,0x20b1d,0x20b1e,0x20b1f,0x35100,0x35101,0x35102,0x35103,0x35104,0x43000,0x50a00,0x50a01,0x50a02,0x50a03,0x50a04,0x50a05,
  0x50a06,0x50a07,0x50a08,0x50a09,0x50a0a,0x50a0b,0x50a0c,0x50a0d,0x50a0e,0x50a0f,0x50a10,0x50a11,0x50a12,0x50a13,0x50a14,0x50a15,
  0x50a16,0x50a17,0x50a18,0x50a19,0x50a1a,0x50a1b,0x50a1c,0x50a1d,0x50a1e,0x50a1f,0x10c0c,0x10c0d,0x01900,0x01901,0x01902,0x01903,
  0x01904,0x01905,0x01906,0x01907,0x01908,0x01909,0x0190a,0x0190b,0x0190c,0x0190d,0x0190e,0x0190f,0x01910,0x01911,0x01912,0x01913,
  0x01914,0x01915,0x01916,0x01917,0x01918,0x01919,0x0191a,0x0191b,0x0191c,0x0191d,0x0191e,0x0191f,0x01920,0x01921,0x01922,0x01923,
  0x01924,0x01925,0x01926,0x01927,0x01928,0x01929,0x0192a,0x0192b,0x0192c,0x0192d,0x0192e,0x0192f,0x01930,0x01931,0x01932,0x01933,
  0x01934,0x01935,0x01936,0x01937,0x01938,0x01939,0x0193a,0x0193b,0x0193c,0x0193d,0x0193e,0x0193f,0x20c00,0x20c01,0x20c02,0x20c03,
  0x20c04,0x10c0e,0x10c0f,0x60a00,0x60a01,0x60a02,0x60a03,0x60a04,0x60a05,0x60a06,0x60a07,0x60a08,0x60a09,0x60a0a,0x60a0b,0x60a0c,
  0x60a0d,0x60a0e,0x60a0f,0x30b00,0x30b01,0x30b02,0x30b03,0x30b04,0x30b05,0x30b06,0x30b07,0x30b08,0x30b09,0x30b0a,0x30b0b,0x30b0c,
  0x30b0d,0x30b0e,0x30b0f,0x11700,0x11701,0x11702,0x11703,0x11704,0x11705,0x11706,0x11707,0x11708,0x11709,0x1170a,0x1170b,0x1170c,
  0x35300,0x35301,0x35302,0x35303,0x35304,0x35305,0x35306,0x35307,0x01a00,0x01a01,0x1160a,0x1160b,0x1160c,0x1160d,0x40b00,0x40b01,
  0x40b02,0x40b03,0x40b04,0x40b05,0x40b06,0x40b07,0x40b08,0x40b09,0x40b0a,0x40b0b,0x40b0c,0x40b0d,0x40b0e,0x40b0f,0x40b10,0x40b11,
  0x40b12,0x40b13,0x40b14,0x40b15,0x40b16,0x40b17,0x40b18,0x40b19,0x40b1a,0x40b1b,0x35200,0x35201,0x35202,0x35203,0x35204,0x35205,
  0x35206,0x35207,0x20c05,0x20c06,0x20c07,0x20c08,0x20c09,0x20c0a,0x20c0b,0x20c0c,0x20c0d,0x20c0e,0x20c0f,0x20c10,0x20c11,0x20c12,
  0x20c13,0x20c14,0x20c15,0x20c16,0x20c17,0x20c18,0x20c19,0x20c1a,0x20c1b,0x20c1c,0x20c1d,0x20c1e,0x20c1f,0x01a02,0x01a03,0x01a04,
  0x01a05,0x01a06,0x01a07,0x01a08,0x01a09,0x01a0a,0x01a0b,0x01a0c,0x01a0d,0x01a0e,0x01a0f,0x01a10,0x01a11,0x01a12,0x01a13,0x01a14,
  0x01a15,0x01a16,0x01a17,0x01a18,0x01a19,0x01a1a,0x01a1b,0x01a1c,0x01a1d,0x01a1e,0x01a1f,0x43200,0x43201,0x43202,0x50b00,0x50b01,
  0x50b02,0x50b03,0x50b04,0x50b05,0x50b06,0x35500,0x35501,0x35502,0x35002,0x35003,0x35004,0x01a20,0x01a21,0x01a22,0x01a23,0x01a24,
  0x01a25,0x01a26,0x01a27,0x01a28,0x01a29,0x01a2a,0x01a2b,0x01a2c,0x01a2d,0x01a2e,0x01a2f,0x01a30,0x01a31,0x43203,0x43204,0x43205,
  0x43206,0x43207,0x43208,0x43209,0x4320a,0x60b00,0x60b01,0x43001,0x01a32,0x01a33,0x01a34,0x01a35,0x01a36,0x01a37,0x01a38,0x01a39,
  0x01a3a,0x01a3b,0x01a3c,0x01a3d,0x01a3e,0x01a3f,0x50b07,0x50b08,0x50b09,0x50b0a,0x50b0b,0x50b0c,0x50b0d,0x50b0e,0x50b0f,0x50b10,
  0x50b11,0x50b12,0x50b13,0x50b14,0x50b15,0x50b16,0x50b17,0x50b18,0x50b19,0x50b1a,0x50b1b,0x1160e,0x1160f,0x30d00,0x30c00,0x35105,
  0x35106,0x35107,0x35005,0x35006,0x35007,0x20e00,0x20e01,0x50b1c,0x50b1d,0x50b1e,0x50b1f,0x10d00,0x10d01,0x10d02,0x10d03,0x10d04,
  0x10d05,0x10d06,0x10d07,0x10d08,0x10d09,0x10d0a,0x10d0b,0x10d0c,0x10d0d,0x10d0e,0x10d0f,0x30c01,0x30c02,0x30c03,0x30c04,0x30c05,
  0x30c06,0x30c07,0x30c08,0x30c09,0x30c0a,0x30c0b,0x30c0c,0x30c0d,0x30c0e,0x30c0f,0x43400,0x43401,0x35400,0x35401,0x35402,0x35403,
  0x35404,0x35405,0x35406,0x35407,0x60e00,0x60e01,0x81800,0x81801,0x81802,0x20d00,0x20d01,0x20d02,0x20d03,0x20d04,0x20d05,0x20d06,
  0x20d07,0x20d08,0x20d09,0x20d0a,0x20d0b,0x20d0c,0x27600,0x27601,0x27602,0x27603,0x1170d,0x1170e,0x1170f,0x35503,0x35504,0x35505,
  0x35506,0x35507,0x81803,0x20d0d,0x20d0e,0x20d0f,0x20d10,0x20d11,0x20d12,0x20d13,0x20d14,0x20d15,0x20d16,0x20d17,0x20d18,0x20d19,
  0x20d1a,0x20d1b,0x20d1c,0x20d1d,0x20d1e,0x20d1f,0x43402,0x43403,0x43404,0x43405,0x43406,0x43407,0x40c00,0x40c01,0x40c02,0x40c03,
  0x40c04,0x40c05,0x40c06,0x40c07,0x40c08,0x40c09,0x40c0a,0x40c0b,0x40c0c,0x40c0d,0x40c0e,0x40c0f,0x40c10,0x40c11,0x40c12,0x40c13,
  0x40c14,0x40c15,0x40c16,0x40c17,0x40c18,0x40c19,0x40c1a,0x40c1b,0x60b02,0x60b03,0x60b04,0x60b05,0x60b06,0x60b07,0x60b08,0x60b09,
  0x60b0a,0x60b0b,0x60b0c,0x60b0d,0x60b0e,0x60b0f,0x43408,0x43409,0x4340a,0x43002,0x43003,0x43004,0x43005,0x43006,0x43007,0x43008,
  0x43009,0x20e02,0x20e03,0x01b00,0x01b01,0x01b02,0x01b03,0x01b04,0x01b05,0x01b06,0x01b07,0x01b08,0x01b09,0x01b0a,0x01b0b,0x01b0c,
  0x01b0d,0x01b0e,0x01b0f,0x01b10,0x01b11,0x01b12,0x01b13,0x01b14,0x01b15,0x01b16,0x01b17,0x01b18,0x01b19,0x01b1a,0x01b1b,0x01b1c,
  0x01b1d,0x01b1e,0x01b1f,0x01b20,0x01b21,0x01b22,0x01b23,0x01b24,0x01b25,0x01b26,0x01b27,0x01b28,0x01b29,0x01b2a,0x01b2b,0x01b2c,
  0x01b2d,0x01b2e,0x01b2f,0x01b30,0x01b31,0x01b32,0x01b33,0x01b34,0x01b35,0x01b36,0x01b37,0x01b38,0x01b39,0x01b3a,0x01b3b,0x01b3c,
  0x01b3d,0x01b3e,0x01b3f,0x30d01,0x30d02,0x30d03,0x30d04,0x30d05,0x30d06,0x30d07,0x30d08,0x30d09,0x30d0a,0x30d0b,0x30d0c,0x30d0d,
  0x30d0e,0x30d0f,0x50c00,0x50c01,0x50c02,0x50c03,0x50c04,0x50c05,0x50c06,0x50c07,0x50c08,0x50c09,0x50c0a,0x50c0b,0x50c0c,0x50c0d,
  0x50c0e,0x50c0f,0x50c10,0x50c11,0x50c12,0x50c13,0x50c14,0x50c15,0x50c16,0x50c17,0x50c18,0x50c19,0x50c1a,0x50c1b,0x50c1c,0x50c1d,
  0x50c1e,0x50c1f,0x81804,0x81805,0x10e00,0x10e01,0x10e02,0x10e03,0x10e04,0x10e05,0x10e06,0x10e07,0x10e08,0x10e09,0x10e0a,0x10e0b,
  0x10e0c,0x10e0d,0x10e0e,0x10e0f,0x40d00,0x40d01,0x40d02,0x40d03,0x40d04,0x40d05,0x40d06,0x40d07,0x40d08,0x40d09,0x40d0a,0x40d0b,
  0x40d0c,0x40d0d,0x40d0e,0x40d0f,0x40d10,0x40d11,0x40d12,0x40d13,0x40d14,0x40d15,0x40d16,0x40d17,0x40d18,0x40d19,0x40d1a,0x40d1b,
  0x27604,0x27605,0x27606,0x27607,0x81b00,0x81b01,0x81b02,0x81806,0x81807,0x81808,0x81809,0x8180a,0x8180b,0x8180c,0x8180d,0x8180e,
  0x8180f,0x81810,0x81811,0x81812,0x81813,0x81814,0x81815,0x81816,0x81817,0x81818,0x81819,0x8181a,0x8181b,0x8181c,0x20e04,0x20e05,
  0x20e06,0x20e07,0x20e08,0x20e09,0x20e0a,0x20e0b,0x20e0c,0x20e0d,0x20e0e,0x20e0f,0x20e10,0x20e11,0x20e12,0x20e13,0x20e14,0x20e15,
  0x20e16,0x20e17,0x20e18,0x20e19,0x20e1a,0x20e1b,0x20e1c,0x20e1d,0x20e1e,0x20e1f,0x60c00,0x60c01,0x60c02,0x60c03,0x60c04,0x60c05,
  0x60c06,0x60c07,0x60c08,0x60c09,0x60c0a,0x60c0b,0x60c0c,0x60c0d,0x60c0e,0x60c0f,0x50d00,0x50d01,0x50d02,0x50d03,0x50d04,0x50d05,
  0x50d06,0x81b03,0x81b04,0x81b05,0x50d07,0x50d08,0x50d09,0x50d0a,0x50d0b,0x10f00,0x50d0c,0x50d0d,0x50d0e,0x60d00,0x60d01,0x10f01,
  0x10f02,0x10f03,0x10f04,0x10f05,0x10f06,0x10f07,0x10f08,0x10f09,0x10f0a,0x10f0b,0x10f0c,0x50d0f,0x50d10,0x50d11,0x50d12,0x50d13,
  0x50d14,0x50d15,0x50d16,0x50d17,0x50d18,0x50d19,0x50d1a,0x50d1b,0x50d1c,0x50d1d,0x50d1e,0x50d1f,0x10f0d,0x10f0e,0x10f0f,0x01c00,
  0x01c01,0x01c02,0x01c03,0x01c04,0x01c05,0x01c06,0x01c07,0x01c08,0x01c09,0x01c0a,0x01c0b,0x01c0c,0x01c0d,0x01c0e,0x01c0f,0x01c10,
  0x01c11,0x01c12,0x01c13,0x01c14,0x01c15,0x01c16,0x01c17,0x01c18,0x01c19,0x01c1a,0x01c1b,0x01c1c,0x01c1d,0x01c1e,0x01c1f,0x01c20,
  0x01c21,0x01c22,0x01c23,0x01c24,0x01c25,0x01c26,0x01c27,0x01c28,0x01c29,0x01c2a,0x01c2b,0x01c2c,0x01c2d,0x01c2e,0x01c2f,0x01c30,
  0x01c31,0x01c32,0x01c33,0x01c34,0x01c35,0x01c36,0x01c37,0x01c38,0x01c39,0x01c3a,0x01c3b,0x01c3c,0x01c3d,0x01c3e,0x01c3f,0x30e00,
  0x30e01,0x30e02,0x30e03,0x30e04,0x30e05,0x30e06,0x30e07,0x30e08,0x30e09,0x30e0a,0x30e0b,0x30e0c,0x30e0d,0x30e0e,0x30e0f,0x81a00,
  0x81a01,0x81a02,0x81a03,0x81a04,0x81a05,0x20f00,0x20f01,0x20f02,0x20f03,0x20f04,0x20f05,0x20f06,0x20f07,0x20f08,0x20f09,0x20f0a,
  0x20f0b,0x20f0c,0x20f0d,0x20f0e,0x20f0f,0x20f10,0x20f11,0x20f12,0x20f13,0x20f14,0x20f15,0x20f16,0x20f17,0x20f18,0x20f19,0x20f1a,
  0x20f1b,0x20f1c,0x20f1d,0x20f1e,0x20f1f,0x30f00,0x30f01,0x30f02,0x30f03,0x81b06,0x81b07,0x81b08,0x81b09,0x81b0a,0x81b0b,0x81b0c,
  0x81b0d,0x81b0e,0x81b0f,0x4340b,0x4340c,0x4340d,0x60d02,0x60d03,0x60d04,0x60d05,0x60d06,0x60d07,0x60d08,0x60d09,0x60d0a,0x60d0b,
  0x60d0c,0x60d0d,0x60d0e,0x60d0f,0x4300a,0x4320b,0x4320c,0x4320d,0x81900,0x81901,0x81902,0x81903,0x81904,0x81905,0x81906,0x81907,
  0x81908,0x81909,0x8190a,0x8190b,0x8190c,0x8190d,0x8190e,0x8190f,0x81910,0x81911,0x81912,0x81913,0x81914,0x81d00,0x60e02,0x60e03,
  0x60e04,0x4300b,0x4300c,0x4300d,0x40e00,0x40e01,0x40e02,0x40e03,0x40e04,0x40e05,0x40e06,0x40e07,0x40e08,0x40e09,0x40e0a,0x40e0b,
  0x40e0c,0x40e0d,0x40e0e,0x40e0f,0x81915,0x81916,0x81917,0x81918,0x81919,0x8191a,0x8191b,0x8191c,0x8191d,0x8191e,0x8191f,0x57200,
  0x40e10,0x40e11,0x40e12,0x40e13,0x40e14,0x40e15,0x40e16,0x40e17,0x40e18,0x40e19,0x40e1a,0x40e1b,0x01d00,0x01d01,0x01d02,0x01d03,
  0x01d04,0x01d05,0x01d06,0x01d07,0x01d08,0x01d09,0x01d0a,0x01d0b,0x01d0c,0x01d0d,0x01d0e,0x01d0f,0x01d10,0x01d11,0x01d12,0x01d13,
  0x01d14,0x01d15,0x01d16,0x01d17,0x01d18,0x01d19,0x01d1a,0x01d1b,0x01d1c,0x01d1d,0x01d1e,0x01d1f,0x01d20,0x01d21,0x01d22,0x01d23,
  0x01d24,0x01d25,0x01d26,0x01d27,0x01d28,0x01d29,0x01d2a,0x01d2b,0x01d2c,0x01d2d,0x01d2e,0x01d2f,0x01d30,0x01d31,0x01d32,0x01d33,
  0x01d34,0x01d35,0x01d36,0x01d37,0x01d38,0x01d39,0x01d3a,0x01d3b,0x01d3c,0x01d3d,0x01d3e,0x01d3f,0x81c00,0x81c01,0x81c02,0x81c03,
  0x81c04,0x81c05,0x81c06,0x81c07,0x81c08,0x81c09,0x81c0a,0x81c0b,0x81c0c,0x81c0d,0x11800,0x11801,0x11802,0x81d01,0x81d02,0x81d03,
  0x81d04,0x81d05,0x81d06,0x81d07,0x81d08,0x81d09,0x81d0a,0x81d0b,0x81d0c,0x81d0d,0x81d0e,0x81d0f,0x81d10,0x50e00,0x50e01,0x50e02,
  0x50e03,0x50e04,0x30f04,0x30f05,0x30f06,0x30f07,0x30f08,0x30f09,0x30f0a,0x30f0b,0x30f0c,0x30f0d,0x30f0e,0x30f0f,0x50e05,0x50e06,
  0x50e07,0x50e08,0x50e09,0x50e0a,0x50e0b,0x50e0c,0x50e0d,0x50e0e,0x50e0f,0x50e10,0x50e11,0x50e12,0x50e13,0x50e14,0x50e15,0x50e16,
  0x50e17,0x50e18,0x50e19,0x50e1a,0x8181d,0x8181e,0x8181f,0x50e1b,0x50e1c,0x50e1d,0x50e1e,0x50e1f,0x81b10,0x81b11,0x81b12,0x81b13,
  0x81b14,0x81b15,0x81b16,0x81b17,0x81b18,0x81b19,0x81b1a,0x81b1b,0x81b1c,0x81b1d,0x81b1e,0x81b1f,0x81a06,0x81a07,0x81a08,0x81a09,
  0x81a0a,0x81a0b,0x81a0c,0x81a0d,0x81a0e,0x81a0f,0x81a10,0x81a11,0x81a12,0x81a13,0x81a14,0x81a15,0x81a16,0x81a17,0x81a18,0x81a19,
  0x81a1a,0x81a1b,0x81a1c,0x81a1d,0x81a1e,0x81a1f,0x11803,0x35600,0x40f00,0x40f01,0x40f02,0x40f03,0x60e05,0x40f04,0x40f05,0x40f06,
  0x40f07,0x40f08,0x40f09,0x40f0a,0x40f0b,0x40f0c,0x40f0d,0x40f0e,0x40f0f,0x40f10,0x40f11,0x40f12,0x40f13,0x40f14,0x40f15,0x40f16,
  0x40f17,0x40f18,0x40f19,0x40f1a,0x40f1b,0x81c0e,0x81c0f,0x35601,0x35602,0x35603,0x35604,0x35605,0x35606,0x35607,0x60e06,0x60e07,
  0x60e08,0x60e09,0x11c00,0x11c01,0x50f00,0x35700,0x35701,0x35702,0x35703,0x35704,0x35705,0x35706,0x35707,0x81d11,0x81d12,0x81d13,
  0x81d14,0x81d15,0x81d16,0x81d17,0x81c10,0x81c11,0x81c12,0x81c13,0x81c14,0x81c15,0x81c16,0x81c17,0x81c18,0x81c19,0x81c1a,0x81c1b,
  0x81c1c,0x81c1d,0x81c1e,0x81c1f,0x11a00,0x11a01,0x11a02,0x60e0a,0x60e0b,0x60e0c,0x60e0d,0x60e0e,0x60e0f,0x11900,0x11901,0x57201,
  0x57202,0x57203,0x57204,0x57205,0x57206,0x57207,0x81d18,0x21800,0x21801,0x21802,0x21803,0x11b00,0x11b01,0x11b02,0x57400,0x57401,
  0x57402,0x57403,0x57404,0x57405,0x57406,0x57407,0x43600,0x43601,0x57000,0x57001,0x57002,0x57003,0x57004,0x01e00,0x01e01,0x01e02,
  0x01e03,0x01e04,0x01e05,0x01e06,0x01e07,0x01e08,0x01e09,0x01e0a,0x01e0b,0x01e0c,0x01e0d,0x01e0e,0x01e0f,0x01e10,0x01e11,0x01e12,
  0x01e13,0x01e14,0x01e15,0x01e16,0x01e17,0x01e18,0x01e19,0x01e1a,0x01e1b,0x01e1c,0x01e1d,0x01e1e,0x01e1f,0x01e20,0x01e21,0x01e22,
  0x01e23,0x01e24,0x01e25,0x01e26,0x01e27,0x01e28,0x01e29,0x01e2a,0x01e2b,0x01e2c,0x01e2d,0x01e2e,0x01e2f,0x01e30,0x01e31,0x01e32,
  0x01e33,0x01e34,0x01e35,0x01e36,0x01e37,0x01e38,0x01e39,0x01e3a,0x01e3b,0x01e3c,0x01e3d,0x01e3e,0x01e3f,0x50f01,0x50f02,0x50f03,
  0x50f04,0x50f05,0x50f06,0x50f07,0x50f08,0x50f09,0x50f0a,0x50f0b,0x50f0c,0x50f0d,0x50f0e,0x50f0f,0x50f10,0x50f11,0x50f12,0x50f13,
  0x50f14,0x50f15,0x50f16,0x50f17,0x50f18,0x50f19,0x50f1a,0x50f1b,0x50f1c,0x50f1d,0x50f1e,0x50f1f,0x57005,0x57006,0x57007,0x21804,
  0x21805,0x43602,0x43603,0x43604,0x43605,0x43606,0x43607,0x43608,0x43609,0x4360a,0x4360b,0x4360c,0x4360d,0x11b03,0x11804,0x11805,
  0x11806,0x11807,0x11808,0x11809,0x1180a,0x1180b,0x1180c,0x1180d,0x1180e,0x1180f,0x11b04,0x11b05,0x11b06,0x11b07,0x81d19,0x81d1a,
  0x81d1b,0x81d1c,0x81d1d,0x81d1e,0x81d1f,0x81e00,0x81e01,0x81e02,0x81e03,0x81e04,0x81e05,0x81e06,0x81e07,0x81e08,0x81e09,0x81e0a,
  0x81e0b,0x81e0c,0x81e0d,0x81e0e,0x81e0f,0x81e10,0x81e11,0x81e12,0x81e13,0x81e14,0x81e15,0x81e16,0x81e17,0x81e18,0x81e19,0x81e1a,
  0x81e1b,0x81e1c,0x81e1d,0x81e1e,0x60f00,0x60f01,0x60f02,0x60f03,0x60f04,0x60f05,0x60f06,0x60f07,0x60f08,0x60f09,0x60f0a,0x60f0b,
  0x60f0c,0x60f0d,0x60f0e,0x60f0f,0x11902,0x11903,0x11904,0x11905,0x11906,0x11907,0x11908,0x11909,0x1190a,0x1190b,0x1190c,0x1190d,
  0x1190e,0x1190f,0x11b08,0x11b09,0x11b0a,0x11a03,0x11a04,0x11a05,0x11a06,0x11a07,0x11a08,0x11a09,0x11a0a,0x11a0b,0x11a0c,0x11a0d,
  0x11a0e,0x11a0f,0x01f00,0x01f01,0x01f02,0x01f03,0x01f04,0x01f05,0x01f06,0x01f07,0x01f08,0x01f09,0x01f0a,0x01f0b,0x01f0c,0x01f0d,
  0x01f0e,0x01f0f,0x01f10,0x01f11,0x01f12,0x01f13,0x01f14,0x01f15,0x01f16,0x01f17,0x01f18,0x01f19,0x01f1a,0x01f1b,0x01f1c,0x01f1d,
  0x01f1e,0x01f1f,0x01f20,0x01f21,0x01f22,0x01f23,0x01f24,0x01f25,0x01f26,0x01f27,0x01f28,0x01f29,0x01f2a,0x01f2b,0x01f2c,0x01f2d,
  0x01f2e,0x01f2f,0x01f30,0x01f31,0x01f32,0x01f33,0x01f34,0x01f35,0x01f36,0x01f37,0x01f38,0x01f39,0x01f3a,0x01f3b,0x01f3c,0x01f3d,
  0x01f3e,0x01f3f,0x81e1f,0x21a00,0x21a01,0x21a02,0x11b0b,0x11b0c,0x11b0d,0x11b0e,0x11b0f,0x21a03,0x21900,0x21901,0x21902,0x21903,
  0x21904,0x21905,0x11d00,0x11d01,0x11d02,0x11d03,0x11d04,0x11d05,0x11d06,0x11d07,0x11d08,0x11d09,0x11d0a,0x11d0b,0x11d0c,0x11d0d,
  0x11d0e,0x11d0f,0x21806,0x21807,0x21808,0x21809,0x2180a,0x2180b,0x2180c,0x2180d,0x2180e,0x2180f,0x21a04,0x21a05,0x21a06,0x21a07,
  0x21a08,0x21a09,0x21a0a,0x21a0b,0x21a0c,0x21a0d,0x21a0e,0x21a0f,0x21a10,0x21a11,0x21a12,0x21a13,0x21a14,0x21a15,0x21a16,0x21a17,
  0x21a18,0x21a19,0x21a1a,0x21a1b,0x21a1c,0x21a1d,0x21a1e,0x21a1f,0x81f00,0x81f01,0x81f02,0x81f03,0x81f04,0x21b00,0x21b01,0x21b02,
  0x21b03,0x21b04,0x21b05,0x21b06,0x81f05,0x81f06,0x81f07,0x81f08,0x81f09,0x81f0a,0x81f0b,0x81f0c,0x81f0d,0x81f0e,0x81f0f,0x81f10,
  0x81f11,0x81f12,0x81f13,0x81f14,0x81f15,0x81f16,0x81f17,0x81f18,0x81f19,0x81f1a,0x81f1b,0x81f1c,0x81f1d,0x81f1e,0x81f1f,0x65400,
  0x11c02,0x11c03,0x11c04,0x11c05,0x11c06,0x11c07,0x11c08,0x11c09,0x11c0a,0x11c0b,0x11c0c,0x11c0d,0x21906,0x21907,0x21908,0x21909,
  0x2190a,0x2190b,0x2190c,0x2190d,0x2190e,0x2190f,0x21910,0x11e00,0x11e01,0x11e02,0x11e03,0x11e04,0x11e05,0x11e06,0x11e07,0x21810,
  0x21811,0x21812,0x21d00,0x21d01,0x11e08,0x11e09,0x11e0a,0x11e0b,0x11e0c,0x21911,0x21912,0x21913,0x21914,0x21915,0x21916,0x21917,
  0x21918,0x21919,0x2191a,0x2191b,0x2191c,0x2191d,0x57600,0x31900,0x57601,0x57602,0x57603,0x57604,0x57605,0x57606,0x57607,0x21b07,
  0x21b08,0x11e0d,0x11e0e,0x11f00,0x11f01,0x11e0f,0x31901,0x31902,0x31903,0x31904,0x31905,0x2191e,0x2191f,0x21b09,0x11f02,0x11f03,
  0x11f04,0x11f05,0x31e00,0x31e01,0x65401,0x65402,0x65403,0x11c0e,0x21b0a,0x21b0b,0x21b0c,0x65600,0x65601,0x65602,0x21d02,0x21d03,
  0x21d04,0x21d05,0x21d06,0x21d07,0x21d08,0x21d09,0x21d0a,0x21d0b,0x21d0c,0x21d0d,0x21d0e,0x21d0f,0x21d10,0x21d11,0x21d12,0x21d13,
  0x21d14,0x21d15,0x21d16,0x21d17,0x21d18,0x21d19,0x21d1a,0x21d1b,0x21d1c,0x21d1d,0x21d1e,0x21d1f,0x31a00,0x31a01,0x31a02,0x41d00,
  0x41d01,0x11c0f,0x41a00,0x21813,0x21814,0x21815,0x21816,0x21817,0x21818,0x21819,0x2181a,0x2181b,0x2181c,0x2181d,0x2181e,0x2181f,
  0x21b0d,0x21b0e,0x21b0f,0x21b10,0x21b11,0x21b12,0x21b13,0x21b14,0x21b15,0x21b16,0x11f06,0x11f07,0x65404,0x11f20,0x11f08,0x11f09,
  0x11f0a,0x65500,0x65501,0x65502,0x65000,0x65001,0x65300,0x65301,0x65302,0x65303,0x65304,0x11f10,0x11f11,0x11f12,0x11f13,0x21f00,
  0x21f01,0x21f02,0x21b17,0x21b18,0x21b19,0x21b1a,0x21b1b,0x21b1c,0x21b1d,0x21b1e,0x21b1f,0x21e00,0x21e01,0x41d02,0x41d03,0x65503,
  0x65504,0x11f14,0x11f15,0x11f16,0x11f17,0x11f18,0x11f19,0x11f1a,0x11f1b,0x11f1c,0x11f1d,0x11f1e,0x11f1f,0x65305,0x65306,0x65307,
  0x41c00,0x41c01,0x41c02,0x21c00,0x21c01,0x21e02,0x21e03,0x21e04,0x21e05,0x21e06,0x21e07,0x21e08,0x21e09,0x21e0a,0x21e0b,0x21e0c,
  0x21e0d,0x21e0e,0x21e0f,0x21e10,0x21e11,0x21e12,0x21e13,0x21e14,0x21e15,0x21e16,0x21e17,0x21e18,0x21e19,0x21e1a,0x21e1b,0x21e1c,
  0x21e1d,0x21e1e,0x21e1f,0x21c02,0x21c03,0x21c04,0x21c05,0x21c06,0x21c07,0x21c08,0x65100,0x65101,0x51b00,0x51b01,0x11f21,0x11f22,
  0x11f23,0x31800,0x31801,0x31802,0x51e00,0x21f03,0x21c09,0x21c0a,0x21c0b,0x21c0c,0x21c0d,0x21c0e,0x21c0f,0x21c10,0x21c11,0x21c12,
  0x21c13,0x21c14,0x21c15,0x21c16,0x21c17,0x21c18,0x21c19,0x21c1a,0x21c1b,0x21c1c,0x21c1d,0x21c1e,0x21c1f,0x11f0b,0x11f0c,0x11f0d,
  0x11f0e,0x11f0f,0x21f04,0x21f05,0x21f06,0x21f07,0x21f08,0x21f09,0x21f0a,0x31803,0x31804,0x31805,0x51f00,0x51f01,0x51f02,0x41d04,
  0x41d05,0x41d06,0x41d07,0x41d08,0x41d09,0x41d0a,0x41d0b,0x41d0c,0x41d0d,0x41d0e,0x31f00,0x31f01,0x31f02,0x65200,0x31d00,0x31d01,
  0x31d02,0x31906,0x31907,0x31908,0x41d0f,0x41d10,0x41d11,0x11f30,0x31c00,0x31c01,0x31c02,0x21f0b,0x11f31,0x11f32,0x11f33,0x11f34,
  0x11f35,0x11f36,0x11f37,0x11f38,0x11f39,0x11f3a,0x11f3b,0x11f3c,0x11f3d,0x11f3e,0x11f3f,0x11f24,0x11f25,0x11f26,0x11f27,0x11f28,
  0x11f29,0x11f2a,0x11f2b,0x11f2c,0x11f2d,0x11f2e,0x11f2f,0x21f0c,0x21f0d,0x21f0e,0x21f0f,0x65700,0x65102,0x65103,0x65104,0x65002,
  0x65003,0x65004,0x65005,0x65006,0x65007,0x31a03,0x31a04,0x31a05,0x31a06,0x31a07,0x31a08,0x31a09,0x31d03,0x31d04,0x31d05,0x31d06,
  0x31d07,0x31d08,0x31d09,0x31d0a,0x31d0b,0x31d0c,0x31d0d,0x31d0e,0x31d0f,0x31806,0x31807,0x31808,0x31809,0x31909,0x3190a,0x3190b,
  0x3190c,0x3190d,0x3190e,0x3190f,0x41800,0x41801,0x3180a,0x3180b,0x3180c,0x21f10,0x21f11,0x21f12,0x21f13,0x21f14,0x3180d,0x3180e,
  0x3180f,0x31e02,0x31e03,0x31e04,0x31e05,0x31e06,0x31e07,0x31e08,0x31e09,0x31e0a,0x31e0b,0x31e0c,0x31e0d,0x31e0e,0x31e0f,0x31a0a,
  0x31a0b,0x31a0c,0x31a0d,0x31a0e,0x31a0f,0x41d12,0x41d13,0x41d14,0x21f15,0x21f16,0x21f17,0x21f18,0x21f19,0x21f1a,0x21f1b,0x21f1c,
  0x21f1d,0x21f1e,0x21f1f,0x21f20,0x21f21,0x21f22,0x21f23,0x21f24,0x21f25,0x21f26,0x31b00,0x31b01,0x31b02,0x31b03,0x31b04,0x31b05,
  0x31b06,0x21f27,0x21f28,0x21f29,0x21f2a,0x21f2b,0x21f2c,0x21f2d,0x21f2e,0x21f2f,0x21f30,0x21f31,0x21f32,0x21f33,0x21f34,0x21f35,
  0x21f36,0x21f37,0x21f38,0x21f39,0x21f3a,0x21f3b,0x41b00,0x41b01,0x31b07,0x31b08,0x31b09,0x31b0a,0x31b0b,0x31b0c,0x31b0d,0x31b0e,
  0x31b0f,0x41d15,0x41d16,0x41d17,0x41d18,0x51f03,0x51f04,0x51f05,0x51f06,0x51f07,0x21f3c,0x21f3d,0x21f3e,0x21f3f,0x65505,0x65506,
  0x65507,0x41d19,0x41d1a,0x41d1b,0x41900,0x41901,0x41902,0x41903,0x41904,0x41905,0x41906,0x41907,0x41908,0x41909,0x4190a,0x4190b,
  0x4190c,0x4190d,0x4190e,0x4190f,0x41910,0x41911,0x41912,0x41913,0x41914,0x41915,0x41916,0x41917,0x41918,0x41919,0x4191a,0x4191b,
  0x41802,0x41803,0x41804,0x41805,0x41806,0x41807,0x41808,0x41809,0x4180a,0x4180b,0x4180c,0x4180d,0x4180e,0x4180f,0x41810,0x41811,
  0x41812,0x41813,0x41814,0x41815,0x41816,0x41817,0x41818,0x41819,0x4181a,0x4181b,0x41b02,0x41b03,0x41b04,0x41a01,0x41a02,0x41a03,
  0x41a04,0x41a05,0x41a06,0x41a07,0x41a08,0x41a09,0x41a0a,0x41a0b,0x41a0c,0x41a0d,0x41a0e,0x41a0f,0x41a10,0x41a11,0x41a12,0x65201,
  0x65202,0x65203,0x31c03,0x31c04,0x41a13,0x41a14,0x41a15,0x41a16,0x41a17,0x41a18,0x41a19,0x41a1a,0x41a1b,0x51800,0x31f03,0x31c05,
  0x31c06,0x31c07,0x31c08,0x31c09,0x31c0a,0x31c0b,0x31c0c,0x31c0d,0x31c0e,0x31c0f,0x41e00,0x41e01,0x41e02,0x41e03,0x41e04,0x41e05,
  0x41e06,0x41e07,0x41e08,0x41e09,0x41e0a,0x41e0b,0x41e0c,0x41e0d,0x41e0e,0x41e0f,0x41e10,0x41e11,0x41e12,0x41e13,0x41e14,0x41e15,
  0x41e16,0x41e17,0x41e18,0x41e19,0x41e1a,0x41e1b,0x41b05,0x41b06,0x41b07,0x41b08,0x41b09,0x41b0a,0x41b0b,0x41b0c,0x41b0d,0x41b0e,
  0x41b0f,0x41b10,0x41b11,0x41b12,0x41b13,0x41b14,0x41b15,0x41b16,0x41b17,0x41b18,0x41b19,0x41b1a,0x41b1b,0x31f04,0x31f05,0x31f06,
  0x31f07,0x31f08,0x31f09,0x31f0a,0x31f0b,0x31f0c,0x31f0d,0x31f0e,0x31f0f,0x31f10,0x31f11,0x31f12,0x31f13,0x31f14,0x31f15,0x31f16,
  0x31f17,0x31f18,0x31f19,0x31f1a,0x31f1b,0x31f1c,0x31f1d,0x31f1e,0x31f1f,0x31f20,0x31f21,0x31f22,0x31f23,0x31f24,0x31f25,0x31f26,
  0x31f27,0x31f28,0x31f29,0x31f2a,0x31f2b,0x31f2c,0x31f2d,0x31f2e,0x31f2f,0x31f30,0x31f31,0x31f32,0x31f33,0x31f34,0x31f35,0x31f36,
  0x31f37,0x31f38,0x31f39,0x31f3a,0x31f3b,0x31f3c,0x31f3d,0x31f3e,0x31f3f,0x41c03,0x41c04,0x41c05,0x41c06,0x41c07,0x41c08,0x65405,
  0x65406,0x65407,0x61900,0x65105,0x65106,0x65107,0x51d00,0x51d01,0x51d02,0x51d03,0x51d04,0x41c09,0x41c0a,0x41c0b,0x41c0c,0x51d05,
  0x51d06,0x51d07,0x65701,0x41c0d,0x41c0e,0x41c0f,0x41c10,0x41c11,0x41c12,0x41c13,0x41c14,0x41c15,0x41c16,0x41c17,0x41c18,0x41c19,
  0x41c1a,0x41c1b,0x51d08,0x51d09,0x51d0a,0x51d0b,0x51d0c,0x51d0d,0x51d0e,0x51d0f,0x51d10,0x51d11,0x51d12,0x51d13,0x51d14,0x51d15,
  0x51d16,0x51d17,0x51d18,0x51d19,0x51d1a,0x51d1b,0x51d1c,0x51d1d,0x51d1e,0x51d1f,0x41f00,0x41f01,0x41f02,0x41f03,0x41f04,0x41f05,
  0x41f06,0x41f07,0x41f08,0x41f09,0x41f0a,0x41f0b,0x41f0c,0x41f0d,0x41f0e,0x41f0f,0x41f10,0x41f11,0x41f12,0x41f13,0x41f14,0x41f15,
  0x41f16,0x41f17,0x41f18,0x41f19,0x41f1a,0x41f1b,0x51801,0x51802,0x51803,0x51804,0x51805,0x51806,0x51807,0x51808,0x51809,0x5180a,
  0x5180b,0x5180c,0x5180d,0x5180e,0x5180f,0x51810,0x51811,0x51812,0x51813,0x51814,0x51815,0x51816,0x51817,0x51818,0x51819,0x5181a,
  0x5181b,0x5181c,0x5181d,0x5181e,0x5181f,0x51900,0x51901,0x51902,0x51903,0x51904,0x51905,0x51906,0x51907,0x51908,0x51909,0x5190a,
  0x5190b,0x5190c,0x5190d,0x5190e,0x5190f,0x51910,0x51911,0x51912,0x51913,0x51914,0x51915,0x51916,0x51917,0x51918,0x51919,0x5191a,
  0x5191b,0x5191c,0x5191d,0x5191e,0x5191f,0x51a00,0x51a01,0x51a02,0x51a03,0x51a04,0x51a05,0x51a06,0x51a07,0x51a08,0x51a09,0x51a0a,
  0x51a0b,0x51a0c,0x51a0d,0x51a0e,0x51a0f,0x51a10,0x51a11,0x51a12,0x51a13,0x51a14,0x51a15,0x51a16,0x51a17,0x51a18,0x51a19,0x51a1a,
  0x51a1b,0x51a1c,0x51a1d,0x51a1e,0x51a1f,0x51e01,0x51e02,0x51e03,0x51e04,0x51e05,0x51e06,0x51e07,0x51e08,0x51e09,0x51e0a,0x51e0b,
  0x51e0c,0x51e0d,0x51e0e,0x51e0f,0x51e10,0x51e11,0x51e12,0x51e13,0x51e14,0x51e15,0x51e16,0x51e17,0x51e18,0x51e19,0x51e1a,0x51e1b,
  0x51e1c,0x51e1d,0x51e1e,0x51e1f,0x51b02,0x51b03,0x51b04,0x65702,0x65703,0x65704,0x65705,0x65706,0x65707,0x51b05,0x51b06,0x51b07,
  0x51b08,0x51b09,0x51b0a,0x51b0b,0x51b0c,0x51b0d,0x51b0e,0x51b0f,0x51b10,0x51b11,0x51b12,0x51b13,0x51b14,0x51b15,0x51b16,0x51b17,
  0x51b18,0x51b19,0x51b1a,0x51b1b,0x51b1c,0x51b1d,0x51b1e,0x51b1f,0x61b00,0x61b01,0x61b02,0x61b03,0x65603,0x65604,0x65605,0x65204,
  0x65205,0x65606,0x65607,0x65206,0x65207,0x61c00,0x61c01,0x61800,0x61801,0x61802,0x61803,0x61804,0x61805,0x61806,0x61807,0x61808,
  0x61809,0x6180a,0x6180b,0x6180c,0x61901,0x61e00,0x61e01,0x61e02,0x61b04,0x61b05,0x61d00,0x61d01,0x61d02,0x61d03,0x61d04,0x61d05,
  0x61d06,0x61d07,0x61d08,0x61d09,0x61d0a,0x61d0b,0x61d0c,0x61d0d,0x61d0e,0x61d0f,0x6180d,0x6180e,0x6180f,0x51c00,0x51c01,0x51c02,
  0x51c03,0x51c04,0x51c05,0x51c06,0x51c07,0x51c08,0x51c09,0x51c0a,0x51c0b,0x51c0c,0x51c0d,0x51c0e,0x51c0f,0x51c10,0x51c11,0x51c12,
  0x51c13,0x51c14,0x51c15,0x51c16,0x51c17,0x51c18,0x51c19,0x51c1a,0x51c1b,0x51c1c,0x51c1d,0x51c1e,0x51c1f,0x61902,0x61903,0x61904,
  0x61905,0x61906,0x61907,0x61908,0x61909,0x6190a,0x6190b,0x6190c,0x6190d,0x6190e,0x6190f,0x51f08,0x51f09,0x51f0a,0x51f0b,0x51f0c,
  0x51f0d,0x51f0e,0x51f0f,0x51f10,0x51f11,0x51f12,0x51f13,0x51f14,0x51f15,0x51f16,0x51f17,0x51f18,0x51f19,0x51f1a,0x51f1b,0x51f1c,
  0x51f1d,0x51f1e,0x51f1f,0x51f20,0x51f21,0x51f22,0x51f23,0x51f24,0x51f25,0x51f26,0x51f27,0x51f28,0x51f29,0x51f2a,0x51f2b,0x51f2c,
  0x51f2d,0x51f2e,0x51f2f,0x51f30,0x51f31,0x51f32,0x51f33,0x51f34,0x51f35,0x51f36,0x51f37,0x51f38,0x51f39,0x51f3a,0x51f3b,0x51f3c,
  0x51f3d,0x51f3e,0x51f3f,0x61e03,0x61e04,0x61e05,0x61e06,0x61e07,0x61e08,0x61e09,0x61e0a,0x61e0b,0x61e0c,0x61e0d,0x61e0e,0x61e0f,
  0x61a00,0x61a01,0x61a02,0x61a03,0x61a04,0x61a05,0x61a06,0x61a07,0x61a08,0x61a09,0x61a0a,0x61a0b,0x61a0c,0x61a0d,0x61a0e,0x61a0f,
  0x61b06,0x61b07,0x61b08,0x61b09,0x61b0a,0x61b0b,0x61b0c,0x61b0d,0x61b0e,0x61b0f,0x61f00,0x61f01,0x61f02,0x61f03,0x61f04,0x61f05,
  0x61f06,0x61f07,0x61f08,0x61f09,0x61f0a,0x61f0b,0x61f0c,0x61f0d,0x61f0e,0x61f0f,0x61f10,0x61f11,0x61f12,0x61f13,0x61f14,0x61f15,
  0x61f16,0x61f17,0x61f18,0x61f19,0x61f1a,0x61f1b,0x61f1c,0x61f1d,0x61f1e,0x61f1f,0x61f20,0x61f21,0x61f22,0x61f23,0x61f24,0x61f25,
  0x61f26,0x61f27,0x61f28,0x61f29,0x61f2a,0x61f2b,0x61f2c,0x61f2d,0x61f2e,0x61f2f,0x61f30,0x61f31,0x61f32,0x61f33,0x61f34,0x61f35,
  0x61f36,0x61f37,0x61f38,0x61f39,0x61f3a,0x61f3b,0x61f3c,0x61f3d,0x61f3e,0x61f3f,0x61c02,0x61c03,0x61c04,0x61c05,0x61c06,0x61c07,
  0x61c08,0x61c09,0x61c0a,0x61c0b,0x61c0c,0x61c0d,0x61c0e,0x61c0f,
};

constexpr int HTB = 128 * 64 * 2;
__device__ __forceinline__ int lds_byte(int r, int c) { const int st = (r >> 4) * 2 + (c >> 5), rr = r & 15, cc = c & 31, ob = rr * 64 + cc * 2; return st * 1024 + (ob ^ (((ob >> 9) & 1) << 5)); }
__device__ __forceinline__ void stage_rc(int b, int& R, int& C) { const int st = b / 1024, sb = b % 1024, swz = sb ^ (((sb >> 9) & 1) << 5); R = (st >> 1) * 16 + swz / 64; C = (st & 1) * 32 + (swz % 64) / 2; }
__device__ __forceinline__ int perm32(int rho) { const int n = rho >> 4, i = rho & 15; return 8 * (i >> 2) + 4 * n + (i & 3); }

#define TS(base, k) do {} while (0)
typedef const __attribute__((address_space(4))) MegaP* kargp;
__device__ __forceinline__ kargp kargs() { kargp kp = (kargp)__builtin_amdgcn_kernarg_segment_ptr(); asm volatile("" : "+s"(kp)); return kp; }
#define CLAIM_NEXT() do { if (w0 && (threadIdx.x & 63) == 0) nx = __hip_atomic_fetch_add(head, 1u, __ATOMIC_RELAXED, __HIP_MEMORY_SCOPE_AGENT); } while (0)
__device__ __forceinline__ int fresh_tid() { int t = threadIdx.x; asm volatile("" : "+v"(t)); return t; }
template <int AUX> __device__ __forceinline__ void g8_unit(LAS unsigned char* lds, rsrc_t rs, unsigned cA, unsigned cB, int pitch, int nt, bool perm, f32x4 (&acc)[2][2][4][2]) {
  const int tid = fresh_tid(), wid = __builtin_amdgcn_readfirstlane(tid >> 6), lane = tid & 63, wr = wid >> 2, wc = wid & 3, fr = lane & 15, fq = lane >> 4;
  unsigned voffA[2], voffB[2];
#pragma unroll
  for (int i = 0; i < 2; ++i) {
    int R, C; stage_rc(tid * 16 + i * 8192, R, C);
    const int Rb = perm ? ((R & ~31) + perm32(R & 31)) : R;
    voffA[i] = (unsigned)(R * pitch + C * 2); voffB[i] = (unsigned)(Rb * pitch + C * 2);
  }
  const unsigned kstep = 128, hstep = 128u * (unsigned)pitch;
  const unsigned ldsw = (unsigned)wid * 1024u;
  const int aoff = lds_byte(wr * 64 + fr, fq * 8), boff = lds_byte(wc * 32 + fr, fq * 8);
#define G8_SA(b, h) (((b) * 2 + (h)) * HTB)
#define G8_SB(b, h) ((4 + (b) * 2 + (h)) * HTB)
#define G8_STA(bufoff, gbase) do { _Pragma("unroll") for (int _i = 0; _i < 2; ++_i) \
    __builtin_amdgcn_raw_ptr_buffer_load_lds(rs, (LAS void*)(lds + (bufoff) + ldsw + _i * 8192), 16, (int)voffA[_i], (int)(gbase), 0, AUX); } while (0)
#define G8_STB(bufoff, gbase) do { _Pragma("unroll") for (int _i = 0; _i < 2; ++_i) \
    __builtin_amdgcn_raw_ptr_buffer_load_lds(rs, (LAS void*)(lds + (bufoff) + ldsw + _i * 8192), 16, (int)voffB[_i], (int)(gbase), 0, 0); } while (0)
#define G8_LDA(dst, b, h) do { _Pragma("unroll") for (int m = 0; m < 4; ++m) _Pragma("unroll") for (int k = 0; k < 2; ++k) dst[m][k] = *(const LAS bf16x8*)(lds + G8_SA(b, h) + aoff + m * 2048 + k * 1024); } while (0)
#define G8_LDB(dst, b, h) do { _Pragma("unroll") for (int n = 0; n < 2; ++n) _Pragma("unroll") for (int k = 0; k < 2; ++k) dst[n][k] = *(const LAS bf16x8*)(lds + G8_SB(b, h) + boff + n * 2048 + k * 1024); } while (0)
#define G8_MMA(ai, bj, At, Bt) do { __builtin_amdgcn_s_setprio(1); _Pragma("unroll") for (int m = 0; m < 4; ++m) _Pragma("unroll") for (int n = 0; n < 2; ++n) _Pragma("unroll") for (int k = 0; k < 2; ++k) \
    acc[ai][bj][m][n] = __builtin_amdgcn_mfma_f32_16x16x32_bf16(Bt[n][k], At[m][k], acc[ai][bj][m][n], 0, 0, 0); __builtin_amdgcn_s_setprio(0); } while (0)
#define G8_WV(n) asm volatile("s_waitcnt vmcnt(" #n ")" ::: "memory")
#define G8_WL(n) asm volatile("s_waitcnt lgkmcnt(" #n ")" ::: "memory")
#define G8_BAR __builtin_amdgcn_s_barrier()
#define G8_SCHED __builtin_amdgcn_sched_barrier(0)
#pragma unroll
  for (int a = 0; a < 2; ++a)
#pragma unroll
    for (int b = 0; b < 2; ++b)
#pragma unroll
      for (int m = 0; m < 4; ++m)
#pragma unroll
        for (int n = 0; n < 2; ++n) acc[a][b][m][n] = (f32x4){0.f, 0.f, 0.f, 0.f};
  bf16x8 At[4][2], B0[2][2], B1[2][2];
  G8_STB(G8_SB(0, 0), cB); G8_STA(G8_SA(0, 0), cA); G8_STB(G8_SB(0, 1), cB + hstep); G8_STA(G8_SA(0, 1), cA + hstep);
  if (wr == 1) G8_BAR;
  G8_WV(4); G8_BAR;
  TS(lds, 0);
  G8_STB(G8_SB(1, 0), cB + kstep); G8_STA(G8_SA(1, 0), cA + kstep); G8_STB(G8_SB(1, 1), cB + hstep + kstep);
  G8_WV(6); G8_BAR;
  for (int t = 0; t < nt - 2; t += 2) {
    const unsigned a1 = cA + (unsigned)(t + 1) * kstep;
    const unsigned a2 = cA + (unsigned)(t + 2) * kstep, b2 = cB + (unsigned)(t + 2) * kstep;
    const unsigned a3 = a2 + kstep, b3 = b2 + kstep;
    G8_LDB(B0, 0, 0); G8_SCHED; G8_LDA(At, 0, 0); G8_STA(G8_SA(1, 1), a1 + hstep);
    G8_WL(8); G8_BAR; G8_WL(0); G8_MMA(0, 0, At, B0); G8_BAR; G8_SCHED;
    G8_LDB(B1, 0, 1); G8_STB(G8_SB(0, 0), b2);
    G8_BAR; G8_WL(0); G8_MMA(0, 1, At, B1); G8_BAR;
    G8_LDA(At, 0, 1); G8_STA(G8_SA(0, 0), a2);
    G8_BAR; G8_WL(0); G8_MMA(1, 0, At, B0); G8_BAR; G8_SCHED;
    G8_STB(G8_SB(0, 1), b2 + hstep);
    G8_WV(6); G8_BAR; G8_MMA(1, 1, At, B1); G8_BAR;
    G8_LDB(B0, 1, 0); G8_SCHED; G8_LDA(At, 1, 0); G8_STA(G8_SA(0, 1), a2 + hstep);
    G8_WL(8); G8_BAR; G8_WL(0); G8_MMA(0, 0, At, B0); G8_BAR; G8_SCHED;
    G8_LDB(B1, 1, 1); G8_STB(G8_SB(1, 0), b3);
    G8_BAR; G8_WL(0); G8_MMA(0, 1, At, B1); G8_BAR;
    G8_LDA(At, 1, 1); G8_STA(G8_SA(1, 0), a3);
    G8_BAR; G8_WL(0); G8_MMA(1, 0, At, B0); G8_BAR; G8_SCHED;
    G8_STB(G8_SB(1, 1), b3 + hstep);
    G8_WV(6); G8_BAR; G8_MMA(1, 1, At, B1); G8_BAR;
  }
  {
    const unsigned al = cA + (unsigned)(nt - 1) * kstep + hstep;
    G8_LDB(B0, 0, 0); G8_WV(0); G8_LDA(At, 0, 0); G8_STA(G8_SA(1, 1), al);
    G8_BAR; G8_WL(0); G8_MMA(0, 0, At, B0); G8_BAR;
    G8_LDB(B1, 0, 1); G8_BAR; G8_WL(0); G8_MMA(0, 1, At, B1); G8_BAR;
    G8_LDA(At, 0, 1); G8_WV(4); G8_BAR; G8_WL(0); G8_MMA(1, 0, At, B0); G8_MMA(1, 1, At, B1); G8_BAR;
  }
  {
    G8_LDB(B0, 1, 0); G8_LDA(At, 1, 0); G8_WV(2); G8_BAR; G8_WL(0); G8_MMA(0, 0, At, B0); G8_BAR;
    G8_LDB(B1, 1, 1); G8_WV(0); G8_BAR; G8_WL(0); G8_MMA(0, 1, At, B1); G8_BAR;
    G8_LDA(At, 1, 1); G8_BAR; G8_WL(0); G8_MMA(1, 0, At, B0); G8_MMA(1, 1, At, B1); G8_BAR;
  }
  if (wr == 0) G8_BAR;
  TS(lds, 1);
#undef G8_SA
#undef G8_SB
#undef G8_STA
#undef G8_STB
#undef G8_LDA
#undef G8_LDB
#undef G8_MMA
#undef G8_WV
#undef G8_WL
#undef G8_BAR
#undef G8_SCHED
}

__device__ __forceinline__ float gelu_tanh(float x) {
  const float t = x * x * (0.044715f * 2.302208198f) + 2.302208198f;
  const float e = __builtin_amdgcn_exp2f(x * t);
  return x - x * __builtin_amdgcn_rcpf(1.0f + e);
}

constexpr int G3_STAGE = 49152;
constexpr int G2_STAGE = 32768;
#define G2_S0 1
#define G2_S1 3
template <int AUX> __device__ __forceinline__ void g2_unit(LAS unsigned char* smem, rsrc_t rs, unsigned oA, unsigned oB, int pitch, int nt, bool perm, f32x4 (&acc)[4][2]) {
  const int tid = fresh_tid(), lane = tid & 63, w = __builtin_amdgcn_readfirstlane(tid >> 6), wr = w >> 2, wc = w & 3;
  const int fr = lane & 15, fq = lane >> 4;
#pragma unroll
  for (int i = 0; i < 4; ++i)
#pragma unroll
    for (int j = 0; j < 2; ++j) acc[i][j] = (f32x4){0.f, 0.f, 0.f, 0.f};
  const int r0 = tid >> 3, ch0 = (tid & 7) ^ (r0 & 7);
  const int rb = perm ? ((r0 & ~31) + perm32(r0 & 31)) : r0;
  const unsigned va0 = (unsigned)(r0 * pitch + ch0 * 16), va1 = va0 + (unsigned)(64 * pitch);
  const unsigned vb0 = (unsigned)(rb * pitch + ch0 * 16), vb1 = vb0 + (unsigned)(64 * pitch);
  LAS unsigned char* ldst = smem + w * 1024;
#define G2_LD(base, voff, buf, kt, o, aux) __builtin_amdgcn_raw_ptr_buffer_load_lds(rs, (LAS void*)(ldst + (buf) * G2_STAGE + (o)), 16, (int)(voff), (int)((base) + (unsigned)(kt) * 128u), 0, aux)
#define G2_LDA(buf, kt, i) G2_LD(oA, (i) ? va1 : va0, buf, kt, (i) * 8192, AUX)
#define G2_LDB(buf, kt, i) G2_LD(oB, (i) ? vb1 : vb0, buf, kt, 16384 + (i) * 8192, AUX)
#pragma unroll
  for (int s = 0; s < 4; ++s) { G2_LDA(s, s, 0); G2_LDA(s, s, 1); G2_LDB(s, s, 0); G2_LDB(s, s, 1); }
  const int sw = fr & 7;
  const int aoff = (wr * 64 + fr) * 128, boff = 16384 + (wc * 32 + fr) * 128;
  const int c0 = (fq ^ sw) * 16, c1 = ((4 + fq) ^ sw) * 16;
  bf16x8 a0[4], b0[2], a1[4], b1[2];
  asm volatile("s_waitcnt vmcnt(12)" ::: "memory");
  __builtin_amdgcn_s_barrier();
  asm volatile("" ::: "memory");
  TS(smem, 0);
#pragma unroll
  for (int j = 0; j < 2; ++j) b0[j] = *(const LAS bf16x8*)(smem + boff + j * 2048 + c0);
#pragma unroll
  for (int i = 0; i < 4; ++i) a0[i] = *(const LAS bf16x8*)(smem + aoff + i * 2048 + c0);
#define G2_BODY(kt, ldb, lda, wv) do { \
    const int buf = (kt) & 3, nbuf = ((kt) + 1) & 3, pbuf = ((kt) + 3) & 3; \
    const LAS unsigned char* sb = smem + buf * G2_STAGE; \
    __builtin_amdgcn_s_waitcnt(0xC07F); \
    __builtin_amdgcn_sched_barrier(0); \
    _Pragma("unroll") for (int i = 0; i < 4; ++i) { \
      if (i == 0) { b1[0] = *(const LAS bf16x8*)(sb + boff + c1); b1[1] = *(const LAS bf16x8*)(sb + boff + 2048 + c1); a1[0] = *(const LAS bf16x8*)(sb + aoff + c1); } \
      if (i == 1) { a1[1] = *(const LAS bf16x8*)(sb + aoff + 2048 + c1); a1[2] = *(const LAS bf16x8*)(sb + aoff + 4096 + c1); a1[3] = *(const LAS bf16x8*)(sb + aoff + 6144 + c1); } \
      if ((ldb) && i == G2_S0) G2_LDB(pbuf, (kt) + 3, 0); \
      if ((ldb) && i == G2_S1) G2_LDB(pbuf, (kt) + 3, 1); \
      __builtin_amdgcn_sched_barrier(0); \
      _Pragma("unroll") for (int j = 0; j < 2; ++j) acc[i][j] = __builtin_amdgcn_mfma_f32_16x16x32_bf16(b0[j], a0[i], acc[i][j], 0, 0, 0); \
      __builtin_amdgcn_sched_barrier(0); \
    } \
    __builtin_amdgcn_s_waitcnt(0xC07F); \
    if ((wv) >= 0) { \
      asm volatile("s_waitcnt vmcnt(%0)" :: "n"((wv) < 0 ? 0 : (wv)) : "memory"); \
      __builtin_amdgcn_s_barrier(); \
      asm volatile("" ::: "memory"); \
    } \
    const LAS unsigned char* sn = smem + nbuf * G2_STAGE; \
    _Pragma("unroll") for (int i = 0; i < 4; ++i) { \
      if ((wv) >= 0) { \
        if (i == 0) { b0[0] = *(const LAS bf16x8*)(sn + boff + c0); b0[1] = *(const LAS bf16x8*)(sn + boff + 2048 + c0); a0[0] = *(const LAS bf16x8*)(sn + aoff + c0); } \
        if (i == 1) { a0[1] = *(const LAS bf16x8*)(sn + aoff + 2048 + c0); a0[2] = *(const LAS bf16x8*)(sn + aoff + 4096 + c0); a0[3] = *(const LAS bf16x8*)(sn + aoff + 6144 + c0); } \
      } \
      if ((lda) && i == G2_S0) G2_LDA(buf, (kt) + 4, 0); \
      if ((lda) && i == G2_S1) G2_LDA(buf, (kt) + 4, 1); \
      __builtin_amdgcn_sched_barrier(0); \
      _Pragma("unroll") for (int j = 0; j < 2; ++j) acc[i][j] = __builtin_amdgcn_mfma_f32_16x16x32_bf16(b1[j], a1[i], acc[i][j], 0, 0, 0); \
      __builtin_amdgcn_sched_barrier(0); \
    } \
  } while (0)
  G2_BODY(0, false, true, 8);
  for (int kt = 1; kt < nt - 4; ++kt) G2_BODY(kt, true, true, 8);
  G2_BODY(nt - 4, true, false, 8);
  G2_BODY(nt - 3, false, false, 4);
  G2_BODY(nt - 2, false, false, 0);
  G2_BODY(nt - 1, false, false, -1);
#undef G2_BODY
#undef G2_LD
#undef G2_LDA
#undef G2_LDB
  asm volatile("s_waitcnt lgkmcnt(0)" ::: "memory");
  __builtin_amdgcn_s_barrier();
  asm volatile("" ::: "memory");
  TS(smem, 1);
}

__device__ __forceinline__ v4u epi_bf16(f32x4 a, f32x4 b, bool isM, f32x4 ba, f32x4 bb) {
  if (isM) {
#pragma unroll
    for (int r = 0; r < 4; ++r) { a[r] = gelu_tanh(rbf(rbf(a[r]) + ba[r])); b[r] = gelu_tanh(rbf(rbf(b[r]) + bb[r])); }
  }
  v4u o; o.x = pack2(a[0], a[1]); o.y = pack2(a[2], a[3]); o.z = pack2(b[0], b[1]); o.w = pack2(b[2], b[3]);
  return o;
}
__device__ __forceinline__ v4u f2u4(f32x4 a) { v4u o; o.x = __float_as_uint(a[0]); o.y = __float_as_uint(a[1]); o.z = __float_as_uint(a[2]); o.w = __float_as_uint(a[3]); return o; }
__device__ __forceinline__ void bias8(const bf16_t* b, f32x4& lo, f32x4& hi) {
  const v4u bb = *(const v4u*)b;
  lo = (f32x4){bf2f(bb.x & 0xffff), bf2f(bb.x >> 16), bf2f(bb.y & 0xffff), bf2f(bb.y >> 16)};
  hi = (f32x4){bf2f(bb.z & 0xffff), bf2f(bb.z >> 16), bf2f(bb.w & 0xffff), bf2f(bb.w >> 16)};
}

__device__ __forceinline__ void task_gemm(const MegaP& p, rsrc_t rs, LAS unsigned char* lds, int kind, int L, bool big, bool alt, int blk, int idx, bool w0, unsigned* head, unsigned& nx) {
  const bool pre = (kind == K_Q || kind == K_QQ) && L == 0;
  unsigned oA, oB; int pitch, nt, pn, ks = 0;
  const bool fine = L == 1 && blk == NBLK - 1;
  const int nsh = big ? 2 : 3, tw = big ? 256 : 128;
  if (kind == K_Q || kind == K_QQ) { pn = idx + (kind == K_Q ? (big ? 16 : 32) : 0); pitch = DM * 2; nt = DM / 64; oA = (unsigned)(L ? WS_XNB : WS_XN); oB = (unsigned)WS_WQKVT; }
  else if (kind == K_M) { pn = idx; pitch = DM * 2; nt = DM / 64; oA = (unsigned)WS_XN1; oB = (unsigned)WS_W1T; }
  else if (kind == K_O) { pn = idx & ((1 << nsh) - 1); ks = idx >> nsh; pitch = HD * 2; const int kl = alt ? HD / OSPLIT2 : fine ? HD / FSPLIT : HD / OSPLIT; nt = kl / 64; oA = (unsigned)WS_O + (unsigned)(ks * kl * 2); oB = (unsigned)WS_WOT + (unsigned)(ks * kl * 2); }
  else { pn = idx & ((1 << nsh) - 1); ks = idx >> nsh; pitch = FF * 2; const int kl = alt ? FF / NSPLIT2 : fine ? FF / FSPLIT : FF / NSPLIT; nt = kl / 64; oA = (unsigned)WS_H + (unsigned)(ks * kl * 2); oB = (unsigned)WS_W2T + (unsigned)(ks * kl * 2); }
  oA += (unsigned)(blk * 128 * pitch); oB += (unsigned)(pn * tw * pitch);
  oA = __builtin_amdgcn_readfirstlane(oA); oB = __builtin_amdgcn_readfirstlane(oB);
  const bool perm = (kind == K_Q) | (kind == K_QQ) | (kind == K_M), isM = kind == K_M;
  const unsigned ldc2 = isM ? FF * 2 : NQKV * 2;
  const unsigned obase = (unsigned)(isM ? WS_H : WS_QKV) + (unsigned)(blk * 128) * ldc2 + (unsigned)(pn * tw) * 2;
  const unsigned sbase = (unsigned)(WS_SLAB + (size_t)(blk >> 1) * SLAB_CHUNK) + (unsigned)ks * (256 * 1024 * 4) + (unsigned)((blk & 1) * 128) * 4096 + (unsigned)(pn * tw) * 4;
  if (big) {
    f32x4 acc[2][2][4][2];
    if (pre) g8_unit<0>(lds, rs, oA, oB, pitch, nt, perm, acc); else g8_unit<AUX_SC1>(lds, rs, oA, oB, pitch, nt, perm, acc);
    CLAIM_NEXT();
    const int tz = fresh_tid();
    const int wid = tz >> 6, lane = tz & 63, wr = wid >> 2, wc = wid & 3, fr = lane & 15, fq = lane >> 4;
    if (perm) {
      const unsigned base = obase + (unsigned)(wr * 64 + fr) * ldc2 + (unsigned)(wc * 32 + fq * 8) * 2;
      f32x4 bv[2][2];
#pragma unroll
      for (int bj = 0; bj < 2; ++bj) {
        bv[bj][0] = bv[bj][1] = (f32x4){0.f, 0.f, 0.f, 0.f};
        if (isM) bias8(kargs()->b1 + pn * 256 + bj * 128 + wc * 32 + fq * 8, bv[bj][0], bv[bj][1]);
      }
#pragma unroll
      for (int ai = 0; ai < 2; ++ai)
#pragma unroll
        for (int m = 0; m < 4; ++m)
#pragma unroll
          for (int bj = 0; bj < 2; ++bj)
            st16(rs, base + (unsigned)(ai * 128 + m * 16) * ldc2 + bj * 256, epi_bf16(acc[ai][bj][m][0], acc[ai][bj][m][1], isM, bv[bj][0], bv[bj][1]));
    } else {
      const unsigned base = sbase + (unsigned)(wr * 64 + fr) * 4096 + (unsigned)(wc * 32 + fq * 4) * 4;
#pragma unroll
      for (int ai = 0; ai < 2; ++ai)
#pragma unroll
        for (int m = 0; m < 4; ++m)
#pragma unroll
          for (int bj = 0; bj < 2; ++bj)
#pragma unroll
            for (int n = 0; n < 2; ++n) st16(rs, base + (unsigned)(ai * 128 + m * 16) * 4096 + bj * 512 + n * 64, f2u4(acc[ai][bj][m][n]));
    }
  } else {
    f32x4 acc[4][2];
    g2_unit<AUX_SC1>(lds, rs, oA, oB, pitch, nt, perm, acc);
    CLAIM_NEXT();
    const int tz = fresh_tid();
    const int wid = tz >> 6, lane = tz & 63, wr = wid >> 2, wc = wid & 3, fr = lane & 15, fq = lane >> 4;
    if (perm) {
      const unsigned base = obase + (unsigned)(wr * 64 + fr) * ldc2 + (unsigned)(wc * 32 + fq * 8) * 2;
      f32x4 bv0 = (f32x4){0.f, 0.f, 0.f, 0.f}, bv1 = bv0;
      if (isM) bias8(kargs()->b1 + pn * 128 + wc * 32 + fq * 8, bv0, bv1);
#pragma unroll
      for (int i = 0; i < 4; ++i) st16(rs, base + (unsigned)(i * 16) * ldc2, epi_bf16(acc[i][0], acc[i][1], isM, bv0, bv1));
    } else {
      const unsigned base = sbase + (unsigned)(wr * 64 + fr) * 4096 + (unsigned)(wc * 32 + fq * 4) * 4;
#pragma unroll
      for (int i = 0; i < 4; ++i)
#pragma unroll
        for (int j = 0; j < 2; ++j) st16(rs, base + (unsigned)(i * 16) * 4096 + j * 64, f2u4(acc[i][j]));
    }
  }
  TS(lds, 2);
}

__device__ __forceinline__ void task_tiny(rsrc_t rs, LAS unsigned char* lds, int kind, int sg, int idx, bool w0, unsigned* head, unsigned& nx) {
  const int tz = fresh_tid();
  const int w = __builtin_amdgcn_readfirstlane(tz >> 6), lane = tz & 63, fr = lane & 15, g = lane >> 4;
  const int row0 = S_TOK - 128 + sg * 32;
  unsigned oA, oB; int pitch, nks, pn, ks = 0;
  if (kind == K_M) { pn = idx; pitch = DM * 2; nks = DM / 64; oA = (unsigned)WS_XN1; oB = (unsigned)WS_W1T; }
  else if (kind == K_O) { pn = idx & 31; ks = idx >> 5; pitch = HD * 2; nks = HD / OSPLIT / 64; oA = (unsigned)WS_O + (unsigned)(ks * (HD / OSPLIT) * 2); oB = (unsigned)WS_WOT + (unsigned)(ks * (HD / OSPLIT) * 2); }
  else { pn = idx & 31; ks = idx >> 5; pitch = FF * 2; nks = FF / NSPLIT / 64; oA = (unsigned)WS_H + (unsigned)(ks * (FF / NSPLIT) * 2); oB = (unsigned)WS_W2T + (unsigned)(ks * (FF / NSPLIT) * 2); }
  static_assert(HD / OSPLIT <= 1024 && FF / NSPLIT <= 1024 && (FF / NSPLIT) % 64 == 0, "tiny unit");
  oA += (unsigned)(row0 * pitch); oB += (unsigned)(pn * 32 * pitch);
  oA = __builtin_amdgcn_readfirstlane(oA); oB = __builtin_amdgcn_readfirstlane(oB);
#pragma unroll
  for (int i = 0; i < 4; ++i)
#pragma unroll
    for (int hf = 0; hf < 2; ++hf) {
      const int r = w * 4 + i;
      const unsigned vo = (unsigned)(r * pitch + ((hf * 64 + lane) ^ (r & 7)) * 16);
      __builtin_amdgcn_raw_ptr_buffer_load_lds(rs, (LAS void*)(lds + r * 2048 + hf * 1024), 16, (int)vo, (int)oA, 0, AUX_SC1);
      __builtin_amdgcn_raw_ptr_buffer_load_lds(rs, (LAS void*)(lds + 65536 + r * 2048 + hf * 1024), 16, (int)vo, (int)oB, 0, 0);
    }
  CLAIM_NEXT();
  const int mi = w & 1, ni = (w >> 1) & 1, kh = w >> 2;
  const int col = pn * 32 + ni * 16 + g * 4, row = row0 + mi * 16 + fr;
  v2u bia = (v2u){0u, 0u};
  if (kind == K_M) bia = *(const v2u*)(kargs()->b1 + col);
  asm volatile("s_waitcnt vmcnt(0)" ::: "memory");
  __builtin_amdgcn_s_barrier();
  asm volatile("" ::: "memory");
  TS(lds, 0);
  f32x4 acc0 = (f32x4){0.f, 0.f, 0.f, 0.f}, acc1 = acc0;
  const LAS unsigned char* pa = lds + (mi * 16 + fr) * 2048, * pb = lds + 65536 + (ni * 16 + fr) * 2048;
  for (int s = 0; s < nks; s += 2) {
    const int o0 = (((kh * nks + s) * 4 + g) ^ (fr & 7)) * 16, o1 = (((kh * nks + s + 1) * 4 + g) ^ (fr & 7)) * 16;
    const bf16x8 a0 = *(const LAS bf16x8*)(pa + o0), b0 = *(const LAS bf16x8*)(pb + o0), a1 = *(const LAS bf16x8*)(pa + o1), b1 = *(const LAS bf16x8*)(pb + o1);
    acc0 = __builtin_amdgcn_mfma_f32_16x16x32_bf16(b0, a0, acc0, 0, 0, 0);
    acc1 = __builtin_amdgcn_mfma_f32_16x16x32_bf16(b1, a1, acc1, 0, 0, 0);
  }
  acc0 += acc1;
  LAS f32x4* red = (LAS f32x4*)(lds + 131072) + (w & 3) * 64 + lane;
  if (kh) *red = acc0;
  asm volatile("s_waitcnt lgkmcnt(0)" ::: "memory");
  __builtin_amdgcn_s_barrier();
  asm volatile("" ::: "memory");
  TS(lds, 1);
  if (!kh) {
    acc0 += *red;
    if (kind == K_M) {
      v2u o;
      o.x = pack2(gelu_tanh(rbf(rbf(acc0[0]) + bf2f(bia.x & 0xffff))), gelu_tanh(rbf(rbf(acc0[1]) + bf2f(bia.x >> 16))));
      o.y = pack2(gelu_tanh(rbf(rbf(acc0[2]) + bf2f(bia.y & 0xffff))), gelu_tanh(rbf(rbf(acc0[3]) + bf2f(bia.y >> 16))));
      st8(rs, (unsigned)WS_H + (unsigned)row * (FF * 2) + (unsigned)col * 2, o);
    } else {
      st16(rs, (unsigned)(WS_SLAB + (size_t)(row >> 8) * SLAB_CHUNK) + (unsigned)ks * (256 * 1024 * 4) + (unsigned)(row & 255) * 4096 + (unsigned)col * 4, f2u4(acc0));
    }
  }
  TS(lds, 2);
}

constexpr float kScale = 0.08838834764831845f;
constexpr int TV_PITCH = 288;
constexpr int TO_PITCH = 272;
typedef short s16x4 __attribute__((ext_vector_type(4)));

__device__ __forceinline__ void task_attn(rsrc_t rs, LAS unsigned char* smem, int L, int seg, int h0, int nh, bool w0, unsigned* head, unsigned& nx) {
  const int tz = fresh_tid();
  const int wid = __builtin_amdgcn_readfirstlane(tz >> 6), lane = tz & 63;
  if (wid >= nh) return;
  const int h = h0 + wid;
  const int fr = lane & 15, g = lane >> 4;
  const unsigned qbase = (unsigned)WS_QKV + (unsigned)(seg * SEG) * (NQKV * 2) + h * DH * 2;
  const unsigned kmem = (unsigned)(WS_KVM + (size_t)L * KVM_LAYER) + (unsigned)(seg * 2 + 0) * (MEMR * HD * 2) + h * DH * 2;
  const unsigned vmem = (unsigned)(WS_KVM + (size_t)L * KVM_LAYER) + (unsigned)(seg * 2 + 1) * (MEMR * HD * 2) + h * DH * 8 * 2;
  LAS unsigned char* VI = smem + wid * 18432;
  LAS unsigned char* QI = VI + 40 * 256;
  {
    const int rl = lane >> 4, pl = lane & 15;
#pragma unroll
    for (int i = 0; i < 2; ++i) { const int r = i * 4 + rl; __builtin_amdgcn_raw_ptr_buffer_load_lds(rs, (LAS void*)(VI + i * 1024), 16, (int)((unsigned)r * (HD * 2) + (unsigned)(pl ^ (r & 7)) * 16), (int)kmem, 0, AUX_SC1); }
#pragma unroll
    for (int i = 0; i < 8; ++i) {
      const int r = i * 4 + rl;
      const unsigned vo = (unsigned)r * (NQKV * 2) + (unsigned)(pl ^ (r & 7)) * 16;
      __builtin_amdgcn_raw_ptr_buffer_load_lds(rs, (LAS void*)(VI + 2048 + i * 1024), 16, (int)vo, (int)(qbase + HD * 2), 0, AUX_SC1);
      __builtin_amdgcn_raw_ptr_buffer_load_lds(rs, (LAS void*)(QI + i * 1024), 16, (int)vo, (int)qbase, 0, AUX_SC1);
    }
  }
  v4u vv[8];
  v2u vm[8];
#pragma unroll
  for (int i = 0; i < 8; ++i) { const int pc = i * 64 + lane, key = pc >> 4, e8 = pc & 15; vv[i] = ld16(rs, qbase + (unsigned)key * (NQKV * 2) + 2 * HD * 2 + e8 * 16); }
#pragma unroll
  for (int et = 0; et < 8; ++et) vm[et] = ld8(rs, vmem + (unsigned)(et * 16 + fr) * 16 + (g & 1) * 8);
  CLAIM_NEXT();
  asm volatile("s_waitcnt vmcnt(16)" ::: "memory");
  f32x4 st[3][2];
#pragma unroll
  for (int a = 0; a < 3; ++a)
#pragma unroll
    for (int b = 0; b < 2; ++b) st[a][b] = (f32x4){0.f, 0.f, 0.f, 0.f};
#pragma unroll
  for (int ks = 0; ks < 4; ++ks) {
    const int po = ((ks * 4 + g) ^ (fr & 7)) * 16;
    bf16x8 kf[3], qf[2];
#pragma unroll
    for (int a = 0; a < 3; ++a) kf[a] = *(const LAS bf16x8*)(VI + (a * 16 + fr) * 256 + po);
#pragma unroll
    for (int b = 0; b < 2; ++b) qf[b] = *(const LAS bf16x8*)(QI + (b * 16 + fr) * 256 + po);
#pragma unroll
    for (int a = 0; a < 3; ++a)
#pragma unroll
      for (int b = 0; b < 2; ++b) st[a][b] = __builtin_amdgcn_mfma_f32_16x16x32_bf16(kf[a], qf[b], st[a][b], 0, 0, 0);
  }
  __builtin_amdgcn_s_waitcnt(0xC07F);
  __builtin_amdgcn_wave_barrier();
#pragma unroll
  for (int i = 0; i < 8; ++i) { const int pc = i * 64 + lane, key = pc >> 4, e8 = pc & 15; *(LAS v4u*)(VI + key * TV_PITCH + e8 * 16) = vv[i]; }
  bf16x8 pf[2][2];
#pragma unroll
  for (int b = 0; b < 2; ++b) {
    const int qi = 8 + b * 16 + fr;
    float sc[3][4]; float mx = -3.0e38f;
#pragma unroll
    for (int a = 0; a < 3; ++a)
#pragma unroll
      for (int r = 0; r < 4; ++r) {
        const int key = a * 16 + g * 4 + r;
        float v = rbf(rbf(st[a][b][r]) * kScale);
        if (key > qi) v = rbf(-1.0e9f);
        if (key >= 40) v = -3.0e38f;
        sc[a][r] = v; mx = fmaxf(mx, v);
      }
    mx = fmaxf(mx, __shfl_xor(mx, 16)); mx = fmaxf(mx, __shfl_xor(mx, 32));
    float sum = 0.f;
#pragma unroll
    for (int a = 0; a < 3; ++a)
#pragma unroll
      for (int r = 0; r < 4; ++r) { const int key = a * 16 + g * 4 + r; const float e = key < 40 ? __expf(sc[a][r] - mx) : 0.f; sc[a][r] = e; sum += e; }
    sum += __shfl_xor(sum, 16); sum += __shfl_xor(sum, 32);
    const float inv = 1.0f / sum;
#pragma unroll
    for (int r = 0; r < 4; ++r) {
      pf[b][0][r] = (short)f2bf(sc[0][r] * inv); pf[b][0][4 + r] = (short)f2bf(sc[1][r] * inv);
      pf[b][1][r] = (short)f2bf(sc[2][r] * inv); pf[b][1][4 + r] = 0;
    }
  }
  __builtin_amdgcn_s_waitcnt(0xC07F);
  __builtin_amdgcn_wave_barrier();
  const int q4 = fr >> 2, c4 = fr & 3;
  const int r0 = (g < 2 ? 0 : 4 * (g - 2)) + q4, r1 = 8 + 4 * g + q4, r2 = (g < 2 ? 24 + 4 * g : 0) + q4;
  v2u ov[8][2];
#pragma unroll
  for (int et = 0; et < 8; ++et) {
    const int cb = et * 32 + c4 * 8;
    const s16x4 t0 = __builtin_amdgcn_ds_read_tr16_b64_v4i16((LAS s16x4*)(VI + r0 * TV_PITCH + cb));
    const s16x4 t1 = __builtin_amdgcn_ds_read_tr16_b64_v4i16((LAS s16x4*)(VI + r1 * TV_PITCH + cb));
    const s16x4 t2 = __builtin_amdgcn_ds_read_tr16_b64_v4i16((LAS s16x4*)(VI + r2 * TV_PITCH + cb));
    v2u a0 = __builtin_bit_cast(v2u, t0);
    const v2u a1 = __builtin_bit_cast(v2u, t1), a2 = __builtin_bit_cast(v2u, t2);
    if (g < 2) a0 = vm[et];
    const v4u va0 = (v4u){a0.x, a0.y, a1.x, a1.y}, va1 = (v4u){a2.x, a2.y, 0u, 0u};
#pragma unroll
    for (int b = 0; b < 2; ++b) {
      f32x4 oc = (f32x4){0.f, 0.f, 0.f, 0.f};
      oc = __builtin_amdgcn_mfma_f32_16x16x32_bf16(__builtin_bit_cast(bf16x8, va0), pf[b][0], oc, 0, 0, 0);
      oc = __builtin_amdgcn_mfma_f32_16x16x32_bf16(__builtin_bit_cast(bf16x8, va1), pf[b][1], oc, 0, 0, 0);
      ov[et][b].x = pack2(oc[0], oc[1]); ov[et][b].y = pack2(oc[2], oc[3]);
    }
  }
  __builtin_amdgcn_s_waitcnt(0xC07F);
  __builtin_amdgcn_wave_barrier();
#pragma unroll
  for (int et = 0; et < 8; ++et)
#pragma unroll
    for (int b = 0; b < 2; ++b) *(LAS v2u*)(VI + (b * 16 + fr) * TO_PITCH + et * 32 + g * 8) = ov[et][b];
  __builtin_amdgcn_s_waitcnt(0xC07F);
  __builtin_amdgcn_wave_barrier();
  const unsigned obase = (unsigned)WS_O + (unsigned)(seg * SEG) * (HD * 2) + h * DH * 2;
#pragma unroll
  for (int i = 0; i < 8; ++i) {
    const int row = i * 4 + g;
    const v4u o = *(const LAS v4u*)(VI + row * TO_PITCH + fr * 16);
    st16(rs, obase + (unsigned)row * (HD * 2) + fr * 16, o);
  }
}

template <bool WIDE>
__device__ __forceinline__ void task_comb(const MegaP& p, rsrc_t rs, int which, int L, int c, int idx, bool w0, unsigned* head, unsigned& nx, int nofine) {
  CLAIM_NEXT();
  const int tz = fresh_tid();
  const int wid = tz >> 6, lane = tz & 63;
  constexpr int RW = WIDE ? 2 * CROWS / 8 : CROWS / 8, NSL = WIDE ? 2 : 8;
  const bool finec = !WIDE && nofine == 0 && L == 1 && c == NBLK - 1;
  const int nsl = WIDE ? 2 : finec ? FSPLIT : (which == 0 ? OSPLIT : NSPLIT);
  static_assert(OSPLIT2 == 2 && NSPLIT2 == 2, "wide combine");
#pragma unroll
  for (int rr = 0; rr < RW; ++rr) {
    if (finec && wid >= 2) return;
    const int row = c * 128 + (finec ? idx * 2 + wid : idx * (RW * 8) + wid * RW + rr);
    const unsigned sb = (unsigned)(WS_SLAB + (size_t)(row >> 8) * SLAB_CHUNK) + (unsigned)(row & 255) * 4096 + lane * 16;
    f32x4 s[4];
#pragma unroll
    for (int j = 0; j < 4; ++j) s[j] = (f32x4){0.f, 0.f, 0.f, 0.f};
    v4u sl[NSL][4];
#pragma unroll
    for (int k = 0; k < NSL; ++k)
      if (k < nsl) {
#pragma unroll
        for (int j = 0; j < 4; ++j) sl[k][j] = ld16(rs, sb + (unsigned)k * (256 * 1024 * 4) + j * 1024);
      }
    v2u res[4];
    if (which == 0 && L == 0) {
#pragma unroll
      for (int j = 0; j < 4; ++j) res[j] = *(const v2u*)(kargs()->x_in + (size_t)row * DM + j * 256 + lane * 4);
    } else {
      const unsigned rb = (unsigned)(which == 0 ? WS_X2 : WS_X1) + (unsigned)row * (DM * 2) + lane * 8;
#pragma unroll
      for (int j = 0; j < 4; ++j) res[j] = ld8(rs, rb + j * 512);
    }
    v2u bia[4];
#pragma unroll
    for (int j = 0; j < 4; ++j) bia[j] = which ? *(const v2u*)(kargs()->b2 + j * 256 + lane * 4) : (v2u){0u, 0u};
#pragma unroll
    for (int k = 0; k < NSL; ++k)
      if (k < nsl) {
#pragma unroll
        for (int j = 0; j < 4; ++j) s[j] += (f32x4){__uint_as_float(sl[k][j].x), __uint_as_float(sl[k][j].y), __uint_as_float(sl[k][j].z), __uint_as_float(sl[k][j].w)};
      }
    float f[16];
#pragma unroll
    for (int j = 0; j < 4; ++j) {
      float y[4] = {rbf(s[j][0]), rbf(s[j][1]), rbf(s[j][2]), rbf(s[j][3])};
      if (which) { y[0] = rbf(y[0] + bf2f(bia[j].x & 0xffff)); y[1] = rbf(y[1] + bf2f(bia[j].x >> 16)); y[2] = rbf(y[2] + bf2f(bia[j].y & 0xffff)); y[3] = rbf(y[3] + bf2f(bia[j].y >> 16)); }
      f[4 * j + 0] = rbf(bf2f(res[j].x & 0xffff) + y[0]); f[4 * j + 1] = rbf(bf2f(res[j].x >> 16) + y[1]);
      f[4 * j + 2] = rbf(bf2f(res[j].y & 0xffff) + y[2]); f[4 * j + 3] = rbf(bf2f(res[j].y >> 16) + y[3]);
    }
    if (which == 1 && L == 1) {
#pragma unroll
      for (int j = 0; j < 4; ++j) { v2u o; o.x = pack2(f[4 * j], f[4 * j + 1]); o.y = pack2(f[4 * j + 2], f[4 * j + 3]); *(v2u*)(kargs()->out + (size_t)row * DM + j * 256 + lane * 4) = o; }
    } else {
      const unsigned xb = (unsigned)(which == 0 ? WS_X1 : WS_X2) + (unsigned)row * (DM * 2) + lane * 8;
#pragma unroll
      for (int j = 0; j < 4; ++j) { v2u o; o.x = pack2(f[4 * j], f[4 * j + 1]); o.y = pack2(f[4 * j + 2], f[4 * j + 3]); st8(rs, xb + j * 512, o); }
      float sm = 0.f;
#pragma unroll
      for (int i = 0; i < 16; ++i) sm += f[i];
#pragma unroll
      for (int m = 32; m >= 1; m >>= 1) sm += __shfl_xor(sm, m);
      const float mean = sm * (1.0f / DM);
      float q = 0.f;
#pragma unroll
      for (int i = 0; i < 16; ++i) { const float d = f[i] - mean; q += d * d; }
#pragma unroll
      for (int m = 32; m >= 1; m >>= 1) q += __shfl_xor(q, m);
      const float mb = rbf(mean), vb = rbf(q * (1.0f / DM));
      const float den = rbf(sqrtf(rbf(vb + 1e-5f)));
      const unsigned nb = (unsigned)(which == 0 ? WS_XN1 : WS_XNB) + (unsigned)row * (DM * 2) + lane * 8;
#pragma unroll
      for (int j = 0; j < 4; ++j) {
        v2u o; o.x = pack2(rbf(f[4 * j] - mb) / den, rbf(f[4 * j + 1] - mb) / den); o.y = pack2(rbf(f[4 * j + 2] - mb) / den, rbf(f[4 * j + 3] - mb) / den);
        st8(rs, nb + j * 512, o);
      }
    }
  }
}

constexpr int TR_TASKS = (DM / 64) * (FF / 64) / 8;
__device__ __forceinline__ void task_tr(const MegaP& p, rsrc_t rs, LAS unsigned char* smem, int which, int idx, bool w0, unsigned* head, unsigned& nx) {
  const int tz = fresh_tid();
  const int wid = tz >> 6, lane = tz & 63;
  CLAIM_NEXT();
  const int R = which ? FF : DM, C = which ? DM : FF;
  kargp kp = kargs();
  const bf16_t* src = which ? kp->w2 : kp->w1;
  const unsigned dst = (unsigned)(which ? WS_W2T : WS_W1T);
  const int tb = idx * 8 + wid, tcn = C / 64, tr = tb / tcn, tc = tb % tcn;
  LAS bf16_t* t = (LAS bf16_t*)(smem + wid * 9216);
  v4u v[8];
#pragma unroll
  for (int i = 0; i < 8; ++i) v[i] = *(const v4u*)(src + (size_t)(tr * 64 + i * 8 + (lane >> 3)) * C + tc * 64 + (lane & 7) * 8);
#pragma unroll
  for (int i = 0; i < 8; ++i) *(LAS v4u*)(t + (i * 8 + (lane >> 3)) * 72 + (lane & 7) * 8) = v[i];
  __builtin_amdgcn_s_waitcnt(0xC07F);
  __builtin_amdgcn_wave_barrier();
#pragma unroll
  for (int i = 0; i < 8; ++i) {
    const int c = i * 8 + (lane >> 3), r8 = lane & 7;
    v4u o;
#pragma unroll
    for (int k = 0; k < 4; ++k) o[k] = (unsigned)t[(r8 * 8 + 2 * k) * 72 + c] | ((unsigned)t[(r8 * 8 + 2 * k + 1) * 72 + c] << 16);
    st16(rs, dst + (unsigned)((tc * 64 + c) * R + tr * 64 + r8 * 8) * 2, o);
  }
}

constexpr int QS_LD = 136;
constexpr int VM_LD = 40;
constexpr int KT_PITCH = 272;
constexpr int VT_PITCH = 288;
constexpr int SS_LD = 100;
constexpr unsigned kSpinCap = 1u << 16;
#define CH_D1 0
#define CH_D2 0
#define CH_D3 0

#define INC1 0
constexpr unsigned kOob = 0x7FFFFF00u;
template <int C> __device__ __forceinline__ unsigned vconst() { unsigned r; asm volatile("v_mov_b32 %0, %1" : "=v"(r) : "n"(C)); return r; }
template <int C> __device__ __forceinline__ v4u vconst4() { return (v4u){vconst<C>(), vconst<C>(), vconst<C>(), vconst<C>()}; }
__device__ __forceinline__ v4u vinv4() { return (v4u){vconst<-1>(), vconst<0>(), vconst<0>(), vconst<0>()}; }
__device__ __forceinline__ v4u sel4(bool c, v4u a, v4u b) { return (v4u){c ? a.x : b.x, c ? a.y : b.y, c ? a.z : b.z, c ? a.w : b.w}; }
template <int N> __device__ __forceinline__ float row_ror(float x) { return __int_as_float(__builtin_amdgcn_update_dpp(0, __float_as_int(x), 0x120 + N, 0xf, 0xf, false)); }
__device__ __forceinline__ float row16_max(float x) { x = fmaxf(x, row_ror<8>(x)); x = fmaxf(x, row_ror<4>(x)); x = fmaxf(x, row_ror<2>(x)); return fmaxf(x, row_ror<1>(x)); }
__device__ __forceinline__ float row16_sum(float x) { x += row_ror<8>(x); x += row_ror<4>(x); x += row_ror<2>(x); return x + row_ror<1>(x); }
__device__ __forceinline__ float x32_sum(float x) { const auto r = __builtin_amdgcn_permlane32_swap(__float_as_uint(x), __float_as_uint(x), false, false); return __uint_as_float(r[0]) + __uint_as_float(r[1]); }
__device__ __forceinline__ bool dw_ok(v4u v) { return (v.x != 0xFFFFFFFFu) & (v.y != 0xFFFFFFFFu) & (v.z != 0xFFFFFFFFu) & (v.w != 0xFFFFFFFFu); }
__device__ __forceinline__ bool tg_ok(v4u v, unsigned tag) { return ((v.x & 0xffff) == tag) & ((v.y & 0xffff) == tag) & ((v.z & 0xffff) == tag) & ((v.w & 0xffff) == tag); }
__device__ __forceinline__ bool d2_ok(v2u v) { return (v.x != 0xFFFFFFFFu) & (v.y != 0xFFFFFFFFu); }
__device__ __forceinline__ unsigned nosent(unsigned x) { return x == 0xFFFFFFFFu ? 0xFFC0FFC0u : x; }

#define STAMP(k) do {} while (0)
#define SPINS(k) do {} while (0)

__device__ __forceinline__ void lds_barrier() { asm volatile("s_waitcnt lgkmcnt(0)" ::: "memory"); __builtin_amdgcn_s_barrier(); asm volatile("" ::: "memory"); }
__device__ __forceinline__ void chain_role(const MegaP& p, rsrc_t rs, unsigned char* smem) {
  float* s_red = (float*)smem;
  bf16_t* s_q = (bf16_t*)(smem + 49152);
  bf16_t* s_k = s_q + 16 * QS_LD;
  bf16_t* s_o = s_k + 16 * QS_LD;
  bf16_t* s_p = s_o + 16 * QS_LD;
  bf16_t* s_vm = s_p + 16 * QS_LD;
  float* s_sc = (float*)(s_vm + 128 * VM_LD);
  float* s_r2 = s_sc + 16 * SS_LD;
  LAS unsigned char* s_kt = (LAS unsigned char*)(s_r2 + 1024);
  LAS unsigned char* s_vt = s_kt + 64 * KT_PITCH;
  const int tid = threadIdx.x, lane = tid & 63, w = __builtin_amdgcn_readfirstlane(tid >> 6), bx = blockIdx.x;
  const int cc = ((bx & 7) | ((bx >> 5) << 3)) * 4 + ((bx >> 3) & 3), h = cc >> 2, j = cc & 3;
  const int cwi = (bx & 7) * 8 + (bx >> 4);
  const unsigned vote = *(const unsigned*)(p.ws + C_VOTE + (lane & 15) * 4) & 3u;
  const int fr = lane & 15, g = lane >> 4;
  const bf16_t* wqkvT = (const bf16_t*)(p.ws + WS_WQKVT);
  const bf16_t* woT = (const bf16_t*)(p.ws + WS_WOT);
  unsigned cap = kSpinCap;
  auto touch_kvm = [&](int L, int sg) {
    const unsigned base = (unsigned)(WS_KVM + (size_t)L * KVM_LAYER) + (unsigned)sg * (2 * MEMR * HD * 2);
    st16(rs, base + (lane < 32 ? (unsigned)((lane >> 2) * HD * 2 + (h * DH + j * 32) * 2 + (lane & 3) * 16) : (unsigned)(MEMR * HD * 2 + (h * DH + j * 32) * 16 + (lane - 32) * 16)), vconst4<0>());
  };

  {
    const v4u z = (v4u){0u, 0u, 0u, 0u};
#pragma unroll
    for (int i = 0; i < 2; ++i) st16(rs, (unsigned)C_XP + cc * 16384 + (i * 512 + tid) * 16, z);
    if (tid < 256) touch_kvm(tid >> 7, (tid >> 6) & 1);
  }
  v2u memreg = (v2u){0u, 0u};
  if (lane < 32) memreg = *(const v2u*)(kargs()->mem0 + (size_t)((lane >> 1) & 7) * DM + cc * 8 + (lane & 1) * 4);
  bf16x8 wq[6][4], wo[2][4];
#pragma unroll
  for (int t = 2; t < 6; ++t)
#pragma unroll
    for (int ks = 0; ks < 4; ++ks)
      wq[t][ks] = *(const bf16x8*)(wqkvT + (size_t)((t >> 1) * HD + h * DH + j * 32 + (t & 1) * 16 + fr) * DM + w * 128 + ks * 32 + g * 8);
  v4u tq[8], to[8];
  {
    const bf16_t* sq = kargs()->wq + h * DH + j * 32;
    const bf16_t* so = kargs()->wo + (size_t)(h * DH) * DM + j * 256;
#pragma unroll
    for (int i = 0; i < 8; ++i) { const int pc = i * 512 + tid; tq[i] = *(const v4u*)(sq + (size_t)(pc >> 2) * HD + (pc & 3) * 8); }
#pragma unroll
    for (int i = 0; i < 8; ++i) { const int pc = i * 512 + tid; to[i] = *(const v4u*)(so + (size_t)(pc >> 5) * DM + (pc & 31) * 8); }
  }
  unsigned polP, polX;
  {
    unsigned bp = 0, bxn = 0; polP = 0; polX = 0;
#pragma unroll
    for (unsigned k = 0; k < 4; ++k) {
      const unsigned long long m = __builtin_amdgcn_ballot_w64(vote == k) & 0xFFFFull;
      const unsigned np = (unsigned)__builtin_popcountll(m & 0x5555ull), nx = (unsigned)__builtin_popcountll(m & 0xAAAAull);
      if (np > bp) { bp = np; polP = k; }
      if (nx > bxn) { bxn = nx; polX = k; }
    }
  }
  const unsigned xpx = polP << 12;
  auto xm_base = [&](int slot, int r) { return (unsigned)(C_XM + (size_t)slot * XM_SLOT) + (unsigned)(r & 1) * 16384u + (((unsigned)(r >> 1) ^ polX) << 12); };
  const unsigned xm_own = (unsigned)((cc >> 4) * 32768 + (cc & 15) * 256);
  const unsigned xq_head = (unsigned)C_XQ + (unsigned)((h >> 3) * 4) * 32768u + (unsigned)(h & 1) * 16384u + (((unsigned)((h & 7) >> 1) ^ polX) << 12);
  {
    const v4u f = (v4u){0xFFFFFFFFu, 0xFFFFFFFFu, 0xFFFFFFFFu, 0xFFFFFFFFu};
    if (tid < 384) st16(rs, xq_head + (tid >= 192 ? (unsigned)XQ_PAR : 0u) + j * 32768 + (tid % 192) * 16, f);
    if (tid < 256) st16(rs, xm_base(1 + (tid >> 7), (tid >> 4) & 7) + xm_own + (tid & 15) * 16, f);
  }
  {
    constexpr int PQ = 64, PO = 528;
    LAS unsigned char* lq = (LAS unsigned char*)smem; LAS unsigned char* lo = lq + 1024 * PQ;
#pragma unroll
    for (int i = 0; i < 8; ++i) { const int pc = i * 512 + tid; *(LAS v4u*)(lq + (pc >> 2) * PQ + (pc & 3) * 16) = tq[i]; }
#pragma unroll
    for (int i = 0; i < 8; ++i) { const int pc = i * 512 + tid; *(LAS v4u*)(lo + (pc >> 5) * PO + (pc & 31) * 16) = to[i]; }
    __syncthreads();
    const int q4 = fr >> 2, c4 = fr & 3;
#pragma unroll
    for (int t = 0; t < 2; ++t)
#pragma unroll
      for (int ks = 0; ks < 4; ++ks) {
        const LAS unsigned char* aq = lq + (w * 128 + ks * 32 + g * 8 + q4) * PQ + t * 32 + c4 * 8;
        const LAS unsigned char* ao = lo + (ks * 32 + g * 8 + q4) * PO + w * 64 + t * 32 + c4 * 8;
        const v2u q0 = __builtin_bit_cast(v2u, __builtin_amdgcn_ds_read_tr16_b64_v4i16((LAS s16x4*)aq)), q1 = __builtin_bit_cast(v2u, __builtin_amdgcn_ds_read_tr16_b64_v4i16((LAS s16x4*)(aq + 4 * PQ)));
        const v2u o0 = __builtin_bit_cast(v2u, __builtin_amdgcn_ds_read_tr16_b64_v4i16((LAS s16x4*)ao)), o1 = __builtin_bit_cast(v2u, __builtin_amdgcn_ds_read_tr16_b64_v4i16((LAS s16x4*)(ao + 4 * PO)));
        const v4u fq4 = (v4u){q0.x, q0.y, q1.x, q1.y}, fo4 = (v4u){o0.x, o0.y, o1.x, o1.y};
        wq[t][ks] = __builtin_bit_cast(bf16x8, fq4); wo[t][ks] = __builtin_bit_cast(bf16x8, fo4);
      }
    __syncthreads();
  }
  for (int i = tid; i < (16 * QS_LD + 128 * VM_LD) / 2; i += 512) ((unsigned*)s_p)[i] = 0;
  for (int i = tid; i < 64 * (KT_PITCH + VT_PITCH) / 4; i += 512) ((LAS unsigned*)s_kt)[i] = 0;

  asm volatile("s_waitcnt vmcnt(0)" ::: "memory");
  __syncthreads();
  if (lane < 32) {
    memreg.x = nosent(memreg.x); memreg.y = nosent(memreg.y);
    st8(rs, xm_base(0, w) + xm_own + lane * 8, memreg);
  }
#pragma unroll
  for (int t = 0; t < 2; ++t)
#pragma unroll
    for (int ks = 0; ks < 4; ++ks) {
      st16(rs, (unsigned)WS_WQKVT + (unsigned)((h * DH + j * 32 + t * 16 + fr) * DM + w * 128 + ks * 32 + g * 8) * 2, __builtin_bit_cast(v4u, wq[t][ks]));
      st16(rs, (unsigned)WS_WOT + (unsigned)((j * 256 + w * 32 + t * 16 + fr) * HD + h * DH + ks * 32 + g * 8) * 2, __builtin_bit_cast(v4u, wo[t][ks]));
    }

  v4u tk[2], tv[2];
  auto load_tok = [&](int L, int sg) {
    const int t3 = fresh_tid();
    const unsigned o = (unsigned)WS_QKV + (unsigned)(sg * SEG + (t3 >> 4)) * (NQKV * 2) + (HD + h * DH) * 2 + (t3 & 15) * 16;
    tk[L] = ld16(rs, o); tv[L] = ld16(rs, o + HD * 2);
  };
  auto store_tok = [&](int L) {
    const int t3 = fresh_tid();
    *(LAS v4u*)(s_kt + (L * 32 + (t3 >> 4)) * KT_PITCH + (t3 & 15) * 16) = tk[L];
    *(LAS v4u*)(s_vt + (L * 32 + (t3 >> 4)) * VT_PITCH + (t3 & 15) * 16) = tv[L];
  };
  auto signal = [&](int sA, int sB) {
    if (tid == 0) {
      if (sA >= 0 && (sA & 3) == 3) __hip_atomic_fetch_add(cnt_ptr(p.ws, K_CH, 0, sA >> 2), vconst<1>(), __ATOMIC_RELAXED, __HIP_MEMORY_SCOPE_AGENT);
      if (sB >= NSEG - 4) __hip_atomic_fetch_add(cntf_ptr(p.ws, K_CH, sB - (NSEG - 4)), vconst<1>(), __ATOMIC_RELAXED, __HIP_MEMORY_SCOPE_AGENT);
      if (sB >= 0 && (sB & 3) == 3) __hip_atomic_fetch_add(cnt_ptr(p.ws, K_CH, 1, sB >> 2), vconst<1>(), __ATOMIC_RELAXED, __HIP_MEMORY_SCOPE_AGENT);
    }
  };

  {
    unsigned spins = 0;
    for (;;) {
        asm volatile("" ::: "memory");
      const unsigned fa = cnt_read(rs, cnt_off(K_Q, 0, 0), lane);
      if (__all(fa >= 64)) break;
      if (++spins > cap) { __builtin_amdgcn_raw_buffer_store_b32(5u, rs, (int)CT_TMO, 0, AUX_SC1); cap = 0; break; }
      __builtin_amdgcn_s_sleep(1);
    }
    load_tok(0, 0);
  }
  int pA = 0, pB = 0;
  int sA = 0, sB = -1;
  int r3 = 0;
  for (int i = 0; i < CH_MAXSTEP; ++i) {
    const int rn = r3 == 2 ? 0 : r3 + 1, rp = rn == 2 ? 0 : rn + 1;
    const unsigned tag = i + 1;
    const int nA = pA + (sA >= 0), nB = pB + (sB >= 0);
    const bool last = sB == NSEG - 1 || i == CH_MAXSTEP - 1;
    {
      v4u a[4];
#pragma unroll
      for (int ks = 0; ks < 4; ++ks) a[ks] = vinv4();
      const unsigned xm_cur = xm_base(r3, bx & 7) + w * 32768 + (g * 16 + fr) * 16;
      unsigned spins = 0;
      if (CH_D1) __builtin_amdgcn_s_sleep(CH_D1);
      for (;;) {
        asm volatile("" ::: "memory");
        bool ok = true;
#pragma unroll
        for (int ks = 0; ks < 4; ++ks) if (!dw_ok(a[ks])) a[ks] = ld16(rs, xm_cur + ks * 1024);
#pragma unroll
        for (int ks = 0; ks < 4; ++ks) ok &= dw_ok(a[ks]);
        if (__all(ok)) break;
        if (++spins > cap) { __builtin_amdgcn_raw_buffer_store_b32(vconst<1>(), rs, (int)CT_TMO, 0, AUX_SC1); cap = 0; break; }
      }
      SPINS(12);
      STAMP(0);
      f32x4 acc[6];
#pragma unroll
      for (int t = 0; t < 6; ++t) acc[t] = (f32x4){0.f, 0.f, 0.f, 0.f};
#pragma unroll
      for (int ks = 0; ks < 4; ++ks) {
        const bf16x8 m = __builtin_bit_cast(bf16x8, a[ks]);
#pragma unroll
        for (int t = 0; t < 4; ++t) acc[t] = __builtin_amdgcn_mfma_f32_16x16x32_bf16(wq[t][ks], m, acc[t], 0, 0, 0);
#pragma unroll
        for (int t = 4; t < 6; ++t) acc[t] = __builtin_amdgcn_mfma_f32_16x16x32_bf16(m, wq[t][ks], acc[t], 0, 0, 0);
      }
#pragma unroll
      for (int t = 0; t < 6; ++t) *(f32x4*)&s_red[((w * 6 + t) * 64 + lane) * 4] = acc[t];
    }
    STAMP(1);
    lds_barrier();
    STAMP(2);
    const bool leader = cc == 0 && w == 7;
    unsigned lfa = 0, lfb = 0;
    bool lra = false, lrb = false;
    if (tid < 384) {
      const int t = w, l2 = lane;
      f32x4 sum = *(f32x4*)&s_red[((0 * 6 + t) * 64 + l2) * 4];
#pragma unroll
      for (int ww = 1; ww < 8; ++ww) sum += *(f32x4*)&s_red[((ww * 6 + t) * 64 + l2) * 4];
      const int which = t >> 1;
      const int off = tid * 8;
      v2u pk; pk.x = nosent(pack2(sum[0], sum[1])); pk.y = nosent(pack2(sum[2], sum[3]));
      st8(rs, xq_head + (i & 1) * (unsigned)XQ_PAR + j * 32768 + off, pk);
      st8(rs, xq_head + ((i & 1) ^ 1) * (unsigned)XQ_PAR + j * 32768 + off, (v2u){vconst<-1>(), vconst<-1>()});
      if (which == 1) {
        const int L = fr >> 3, sg = L ? sB : sA;
        if (sg >= 0) st8(rs, (unsigned)(WS_KVM + (size_t)L * KVM_LAYER) + (unsigned)sg * (2 * MEMR * HD * 2) + (unsigned)((fr & 7) * HD + h * DH + j * 32 + (t & 1) * 16 + g * 4) * 2, pk);
      } else if (which == 2) {
        const int L = g >> 1, sg = L ? sB : sA;
        if (sg >= 0) st8(rs, (unsigned)(WS_KVM + (size_t)L * KVM_LAYER) + (unsigned)sg * (2 * MEMR * HD * 2) + (unsigned)(MEMR * HD * 2) + (unsigned)((h * DH + j * 32 + (t & 1) * 16 + fr) * 8 + (g & 1) * 4) * 2, pk);
      }
    } else {
      const int sg = w == 6 ? sA : sB;
      if (sg >= 0 && sg + 2 < NSEG) touch_kvm(w - 6, sg + 2);
      { const int q = tid - 384; st16(rs, xm_base(rp, q >> 4) + xm_own + (q & 15) * 16, vconst4<-1>()); }
      if (leader) {
        lfa = __builtin_amdgcn_raw_buffer_load_b32(rs, (int)(cnt_off(K_Q, 0, (nA >> 2) & 15) + (lane & 15) * 256), 0, AUX_SC1);
        lfb = __builtin_amdgcn_raw_buffer_load_b32(rs, (int)(cnt_off(K_Q, 1, (nB >> 2) & 15) + (lane & 15) * 256), 0, AUX_SC1);
      }
    }
    STAMP(3);
    if (last) {
      asm volatile("s_waitcnt vmcnt(0)" ::: "memory");
      lds_barrier();
      signal(sA, sB);
      break;
    }
    if (sA >= 0) store_tok(0);
    if (sB >= 0) store_tok(1);
    {
      v4u v[2];
#pragma unroll
      for (int k = 0; k < 2; ++k) v[k] = vinv4();
      const int t2 = fresh_tid();
      const unsigned xq_rd = xq_head + (i & 1) * (unsigned)XQ_PAR + (w >= 3 ? 32768u - 192u * 16u : 0u) + t2 * 16;
      unsigned spins = 0;
      if (CH_D2) __builtin_amdgcn_s_sleep(CH_D2);
      if (w < 6) {
        for (;;) {
        asm volatile("" ::: "memory");
          bool ok = true;
#pragma unroll
          for (int k = 0; k < 2; ++k) if (!dw_ok(v[k])) v[k] = ld16(rs, xq_rd + k * 65536);
#pragma unroll
          for (int k = 0; k < 2; ++k) ok &= dw_ok(v[k]);
          if (__all(ok)) break;
          if (++spins > cap) { __builtin_amdgcn_raw_buffer_store_b32(vconst<2>(), rs, (int)CT_TMO, 0, AUX_SC1); cap = 0; break; }
        }
      }
      if (leader) asm volatile("s_waitcnt vmcnt(2)" ::: "memory"); else asm volatile("s_waitcnt vmcnt(0)" ::: "memory");
      SPINS(13);
      STAMP(4);
      if (w < 6) {
        const int hi = w >= 3, which = w - 3 * hi, l2 = t2 & 63, t16 = (l2 >> 5) * 16, pf = (l2 * 2) & 15, pg = (l2 >> 3) & 3;
#pragma unroll
        for (int k = 0; k < 2; ++k) {
          const int jp = hi + 2 * k;
          const v2u o0 = (v2u){v[k].x, v[k].y}, o1 = (v2u){v[k].z, v[k].w};
          if (which < 2) { bf16_t* d = &(which == 0 ? s_q : s_k)[pf * QS_LD + jp * 32 + t16 + pg * 4]; *(v2u*)d = o0; *(v2u*)(d + QS_LD) = o1; }
          else { bf16_t* d = &s_vm[(jp * 32 + t16 + pf) * VM_LD + pg * 4]; *(v2u*)d = o0; *(v2u*)(d + VM_LD) = o1; }
        }
      }
    }
    lds_barrier();
    __builtin_amdgcn_s_waitcnt(0x0F70);
    if (leader) {
      bool rA = nA < NSEG, rB = nB < NSEG;
      lra = __all(row16_sum_u(lfa) >= 64); lrb = __all(row16_sum_u(lfb) >= 64);
      unsigned spins = 0;
      for (;;) {
        asm volatile("" ::: "memory");
        const bool a = rA && ((nA & 3) != 0 || lra), b = rB && ((nB & 3) != 0 || lrb);
        if (a | b) { rA = a; rB = b; break; }
        if (++spins > cap) { __builtin_amdgcn_raw_buffer_store_b32(vconst<2>(), rs, (int)CT_TMO, 0, AUX_SC1); cap = 0; rA = rB = false; break; }
        const unsigned fa = cnt_read(rs, cnt_off(K_Q, 0, (nA >> 2) & 15), lane), fb = cnt_read(rs, cnt_off(K_Q, 1, (nB >> 2) & 15), lane);
        lra = __all(fa >= 64); lrb = __all(fb >= 64);
      }
      __builtin_amdgcn_raw_buffer_store_b32((tag << 8) | 1u | (rA ? 2u : 0u) | (rB ? 4u : 0u), rs, (int)(C_CTL + lane * 128), 0, AUX_SC1);
    }
    const bool preA = nA < NSEG && (nA & 3) != 0, preB = nB < NSEG && (nB & 3) != 0;
    if (preA | preB) { load_tok(0, preA ? nA : 0); load_tok(1, preB ? nB : 0); }
    signal(sA, sB);
    if (i == 0 && tid == 64) __hip_atomic_fetch_add(cnt_ptr(p.ws, K_TR, 0, 1), vconst<1>(), __ATOMIC_RELAXED, __HIP_MEMORY_SCOPE_AGENT);
    STAMP(5);
    if (w < 5) {
      f32x4 st = (f32x4){0.f, 0.f, 0.f, 0.f};
#pragma unroll
      for (int ks = 0; ks < 4; ++ks) {
        const bf16x8 qf = *(const bf16x8*)&s_q[fr * QS_LD + ks * 32 + g * 8];
        bf16x8 kf;
        if (w == 0) kf = *(const bf16x8*)&s_k[fr * QS_LD + ks * 32 + g * 8];
        else kf = *(const LAS bf16x8*)(s_kt + ((w - 1) * 16 + fr) * KT_PITCH + ks * 64 + g * 16);
        st = __builtin_amdgcn_mfma_f32_16x16x32_bf16(kf, qf, st, 0, 0, 0);
      }
      *(f32x4*)&s_sc[fr * SS_LD + w * 16 + g * 4] = st;
    }
    lds_barrier();
    STAMP(6);
    if (tid < 256) {
      const int q = tid >> 4, sub = tid & 15, L = q >> 3;
      typedef float f32x2v __attribute__((ext_vector_type(2)));
      const f32x2v rt = *(const f32x2v*)&s_sc[q * SS_LD + 16 + L * 32 + sub * 2];
      const float rm = s_sc[q * SS_LD + L * 8 + (sub & 7)];
      const float s0 = rbf(rbf(rt[0]) * kScale), s1 = rbf(rbf(rt[1]) * kScale);
      const float s2 = sub < 8 ? rbf(rbf(rm) * kScale) : -3.0e38f;
      const float mx = row16_max(fmaxf(fmaxf(s0, s1), s2));
      const float e0 = __expf(s0 - mx), e1 = __expf(s1 - mx), e2 = sub < 8 ? __expf(s2 - mx) : 0.f;
      const float inv = 1.0f / row16_sum(e0 + e1 + e2);
      *(unsigned*)&s_p[q * QS_LD + L * 32 + sub * 2] = pack2(e0 * inv, e1 * inv);
      if (sub < 8) s_p[q * QS_LD + 64 + L * 8 + sub] = f2bf(e2 * inv);
    }
    lds_barrier();
    {
      f32x4 oc = (f32x4){0.f, 0.f, 0.f, 0.f};
#pragma unroll
      for (int ks = 0; ks < 3; ++ks) {
        const bf16x8 pf = *(const bf16x8*)&s_p[fr * QS_LD + ks * 32 + g * 8];
        bf16x8 vf;
        if (ks < 2) {
          const LAS unsigned char* a = s_vt + (ks * 32 + g * 8 + (fr >> 2)) * VT_PITCH + w * 32 + (fr & 3) * 8;
          const v2u t0 = __builtin_bit_cast(v2u, __builtin_amdgcn_ds_read_tr16_b64_v4i16((LAS s16x4*)a)), t1 = __builtin_bit_cast(v2u, __builtin_amdgcn_ds_read_tr16_b64_v4i16((LAS s16x4*)(a + 4 * VT_PITCH)));
          vf = __builtin_bit_cast(bf16x8, ((v4u){t0.x, t0.y, t1.x, t1.y}));
        } else vf = *(const bf16x8*)&s_vm[(w * 16 + fr) * VM_LD + g * 8];
        oc = __builtin_amdgcn_mfma_f32_16x16x32_bf16(vf, pf, oc, 0, 0, 0);
      }
      v2u ov; ov.x = pack2(oc[0], oc[1]); ov.y = pack2(oc[2], oc[3]);
      *(v2u*)&s_o[fr * QS_LD + w * 16 + g * 4] = ov;
    }
    lds_barrier();
    STAMP(7);
    {
      f32x4 acc[2] = {(f32x4){0.f, 0.f, 0.f, 0.f}, (f32x4){0.f, 0.f, 0.f, 0.f}};
#pragma unroll
      for (int ks = 0; ks < 4; ++ks) {
        const bf16x8 a = *(const bf16x8*)&s_o[fr * QS_LD + ks * 32 + g * 8];
#pragma unroll
        for (int t = 0; t < 2; ++t) acc[t] = __builtin_amdgcn_mfma_f32_16x16x32_bf16(wo[t][ks], a, acc[t], 0, 0, 0);
      }
      const unsigned ph = tag & 1;
#pragma unroll
      for (int t = 0; t < 2; ++t) {
        v4u o;
#pragma unroll
        for (int r = 0; r < 4; ++r) o[r] = (__float_as_uint(acc[t][r]) & ~1u) | ph;
        const int c2 = j * 32 + w * 4 + t * 2 + (g >> 1);
        st16(rs, (unsigned)C_XP + (((h * 128 + c2) * 512) ^ xpx) + (g & 1) * 256 + fr * 16, o);
      }
    }
    STAMP(8);
    STAMP(9);
    unsigned cw = 0;
    {
      const int hg = tid >> 5, piece = tid & 31;
      const unsigned ph = tag & 1;
      v4u v[2];
      bool okk[2] = {false, false};
      unsigned spins = 0;
      if (CH_D3) __builtin_amdgcn_s_sleep(CH_D3);
      for (;;) {
        asm volatile("" ::: "memory");
        bool ok = true;
        if (cw == 0) { cw = __builtin_amdgcn_raw_buffer_load_b32(rs, (int)(C_CTL + cwi * 128), 0, AUX_SC1); if ((cw >> 8) != tag) cw = 0; }
#pragma unroll
        for (int k = 0; k < 2; ++k) if (!okk[k]) v[k] = ld16(rs, (unsigned)C_XP + ((((hg + 16 * k) * 128 + cc) * 512) ^ xpx) + piece * 16);
#pragma unroll
        for (int k = 0; k < 2; ++k) { okk[k] = (((v[k].x & v[k].y & v[k].z & v[k].w) & 1) == ph) & (((v[k].x | v[k].y | v[k].z | v[k].w) & 1) == ph); ok &= okk[k]; }
        ok &= cw != 0;
        if (__all(ok)) break;
        if (++spins > cap) { __builtin_amdgcn_raw_buffer_store_b32(vconst<3>(), rs, (int)CT_TMO, 0, AUX_SC1); cap = 0; break; }
      }
      SPINS(14);
      STAMP(10);
      f32x4 sum;
#pragma unroll
      for (int r = 0; r < 4; ++r) { sum[r] = __uint_as_float(v[0][r] & ~1u) + __uint_as_float(v[1][r] & ~1u); sum[r] = x32_sum(sum[r]); }
      if (lane < 32) *(f32x4*)&s_r2[(w * 32 + lane) * 4] = sum;
    }
    lds_barrier();
    if (lane < 32) {
      const int row = lane >> 1, half = lane & 1, L = row >> 3, pc = half * 16 + row;
      f32x4 sum = *(f32x4*)&s_r2[pc * 4];
#pragma unroll
      for (int ww = 1; ww < 8; ++ww) sum += *(f32x4*)&s_r2[(ww * 32 + pc) * 4];
      v2u o; o.x = nosent(pack2(sum[0], sum[1])); o.y = nosent(pack2(sum[2], sum[3]));
      if ((L ? sB : sA) >= 0) memreg = o;
      st8(rs, xm_base(rn, w) + xm_own + lane * 8, memreg);
    }
    r3 = rn;
    STAMP(11);
    cw = __builtin_amdgcn_readfirstlane(cw);
    pA = nA; pB = nB;
    sA = (cw & 2) ? nA : -1; sB = (cw & 4) ? nB : -1;
    if (sA >= 0 && !preA) load_tok(0, sA);
    if (sB >= 0 && !preB) load_tok(1, sB);
  }
}

constexpr int LDS_MAIN = 3 * G3_STAGE;
constexpr int LDS_TOTAL = LDS_MAIN + 256;

__device__ __forceinline__ void task_deps(unsigned code, int lane, unsigned& off, unsigned& need) {
  const int kind = code >> 16, L = (code >> 12) & 1, c = (code >> 8) & 15;
  const bool big = (code >> 13) & 1;
  off = CT_CNT; need = 0;
  if ((code >> 15) & 1) {
    if (kind == K_T) { if (lane < 3) { off = lane == 0 ? cnt_off(K_Q, 1, NBLK - 1) : lane == 1 ? cntf_off(K_CH, c) : cnt_off(K_QQ, 1, NBLK - 1); need = lane == 0 ? 64 : lane == 1 ? CH_WGS : 32; } }
    else if (lane == 0) { off = cntf_off(kind - 1, c); need = kind == K_O ? 16 : kind == K_C1 ? 32 * OSPLIT : kind == K_M ? 4 : kind == K_N ? FF / 32 : 32 * NSPLIT; }
    else if (lane == 2 && (kind == K_M || kind == K_N)) { off = cnt_off(K_TR, kind == K_N, 0); need = TR_TASKS; }
    else if (lane == 2 && kind == K_O) { off = cnt_off(K_TR, 0, 1); need = CH_WGS; }
  } else if (kind == K_T) {
    if (lane < 3) { off = lane == 0 ? cnt_off(K_Q, L, c) : lane == 1 ? (L == 1 && c == NBLK - 1 ? cntf_off(K_CH, (code >> 4) & 3) : cnt_off(K_CH, L, c)) : cnt_off(K_QQ, L, c); need = lane == 0 ? 64 : lane == 1 ? CH_WGS : 32; }
  } else if (lane < (big ? 2 : 1)) {
    const int cb = c + lane;
    if (kind == K_Q || kind == K_QQ) { off = cnt_off(K_C2, 0, cb); need = L == 1 ? 16 : 0; }
    else if (kind != K_TR) { off = cnt_off(kind - 1, L, cb); const bool ff = L == 1 && c == NBLK - 1; need = kind == K_O ? (ff ? 64 : 16) : kind == K_C1 ? 8 * (ff ? FSPLIT : OSPLIT) : kind == K_M ? (ff ? 64 : 16) : kind == K_N ? 28 : 8 * (ff ? FSPLIT : NSPLIT); }
  } else if (lane == 2 && (kind == K_M || kind == K_N)) { off = cnt_off(K_TR, kind == K_N, 0); need = TR_TASKS; }
  else if (lane == 2 && (kind == K_QQ || kind == K_O)) { off = cnt_off(K_TR, 0, 1); need = CH_WGS; }
}

__global__ void __launch_bounds__(512, 2) k_mega(MegaP p) {
  extern __shared__ __attribute__((aligned(16))) unsigned char smem[];
  const rsrc_t rs = __builtin_amdgcn_make_buffer_rsrc(p.ws, 0, (int)WS_END, 0x00020000);
  const int tid = threadIdx.x;
  if (blockIdx.x < CH_WGS) { chain_role(p, rs, smem); __syncthreads(); }
  LAS volatile unsigned* s_task = (LAS volatile unsigned*)((LAS unsigned char*)smem + LDS_MAIN);
  const bool w0 = __builtin_amdgcn_readfirstlane(tid >> 6) == 0;
  int lst = (WPOOL1 > 0 && (int)blockIdx.x >= 256 - WPOOL1) ? 1 : 0;
  bool both = false;
  unsigned* head = (unsigned*)(p.ws + CT_HEAD) + lst * 16;
  if (w0) {
    unsigned t = 0;
    if ((tid & 63) == 0) t = __hip_atomic_fetch_add(head, 1u, __ATOMIC_RELAXED, __HIP_MEMORY_SCOPE_AGENT);
    t = __builtin_amdgcn_readfirstlane(t);
    s_task[0] = t; s_task[1] = 0;
  }
  __syncthreads();
  unsigned have = 0;
  for (unsigned it = 0;; ++it) {
    const unsigned tr = __builtin_amdgcn_readfirstlane(s_task[(it & 1) * 2]), rdy = __builtin_amdgcn_readfirstlane(s_task[(it & 1) * 2 + 1]);
    if (tr >= (unsigned)(lst ? NTASKS - NTASK0 : NTASK0)) {
      if (both) break;
      both = true; lst ^= 1; head = (unsigned*)(p.ws + CT_HEAD) + lst * 16;
      __syncthreads();
      if (w0) {
        unsigned t = 0;
        if ((tid & 63) == 0) t = __hip_atomic_fetch_add(head, 1u, __ATOMIC_RELAXED, __HIP_MEMORY_SCOPE_AGENT);
        t = __builtin_amdgcn_readfirstlane(t);
        s_task[((it + 1) & 1) * 2] = t; s_task[((it + 1) & 1) * 2 + 1] = 0;
      }
      __syncthreads();
      continue;
    }
    const unsigned ti = tr + (lst ? NTASK0 : 0);
    const unsigned code = kTasks[ti];
    const int kind = code >> 16, L = (code >> 12) & 1, c = (code >> 8) & 15, idx = code & 255;
    const bool big = (code >> 13) & 1, alt = (code >> 14) & 1, fast = (code >> 15) & 1;
    if (!rdy) {
      if (w0) {
        const int lane = fresh_tid() & 63;
        unsigned off, need; task_deps(code, lane >> 4, off, need);
        unsigned* tmo = (unsigned*)p.ws;
        const unsigned long long t0 = __builtin_amdgcn_s_memrealtime();
        unsigned polls = 0;
        while (!__all(cnt_read(rs, off, lane) >= need)) {
          asm volatile("" ::: "memory");
          if ((++polls & 63u) == 0u) {
            if (__builtin_amdgcn_readfirstlane(__hip_atomic_load(tmo, __ATOMIC_RELAXED, __HIP_MEMORY_SCOPE_AGENT)) != 0u) break;
            if (__builtin_amdgcn_s_memrealtime() - t0 > 3000000ull) { __hip_atomic_store(tmo, 4u, __ATOMIC_RELAXED, __HIP_MEMORY_SCOPE_AGENT); break; }
          }
          __builtin_amdgcn_s_sleep(2);
        }
      }
      __syncthreads();
    }
    {
      const unsigned cls = !big ? 0u : (kind == K_QQ || kind == K_O) ? 1u : kind == K_M ? 2u : kind == K_N ? 4u : 0u;
      if (cls & ~have) {
        if (w0) __builtin_amdgcn_fence(__ATOMIC_ACQUIRE, "agent");
        __syncthreads();
        have |= cls;
      }
    }
    unsigned nx = 0;
    const bool fine = !fast && L == 1 && c == NBLK - 1;
    if (kind == K_T) task_attn(rs, (LAS unsigned char*)smem, L, fast ? NSEG - 4 + c : c * 4 + (idx >> (fine ? 4 : 2)), fast ? idx * 2 : fine ? (idx & 15) * 2 : (idx & 3) * 8, (fast | fine) ? 2 : 8, w0, head, nx);
    else if (kind == K_TR) task_tr(p, rs, (LAS unsigned char*)smem, L, idx, w0, head, nx);
    else if (kind == K_C1 || kind == K_C2) { if (alt) task_comb<true>(p, rs, kind == K_C2, L, c, idx, w0, head, nx, 1); else task_comb<false>(p, rs, kind == K_C2, L, fast ? NBLK - 1 : c, fast ? c * 4 + idx : idx, w0, head, nx, fast ? 1 : 0); }
    else if (fast) task_tiny(rs, (LAS unsigned char*)smem, kind, c, idx, w0, head, nx);
    else task_gemm(p, rs, (LAS unsigned char*)smem, kind, L, big, alt, c, idx, w0, head, nx);
    if (w0) {
      const int lane = fresh_tid() & 63;
      const unsigned tn = __builtin_amdgcn_readfirstlane(nx);
      unsigned ok = 0;
      if (tn < (unsigned)(lst ? NTASKS - NTASK0 : NTASK0)) {
        unsigned off, need; task_deps(kTasks[tn + (lst ? NTASK0 : 0)], lane >> 4, off, need);
        ok = __all(cnt_read(rs, off, lane) >= need) ? 1u : 0u;
      }
      s_task[((it + 1) & 1) * 2] = tn; s_task[((it + 1) & 1) * 2 + 1] = ok;
    }
    asm volatile("s_waitcnt vmcnt(0) lgkmcnt(0)" ::: "memory");
    __syncthreads();
    if (w0) {
      const int lane = fresh_tid() & 63;
      const unsigned inc = alt ? (kind == K_O ? (unsigned)OSPLIT : kind == K_N ? (unsigned)NSPLIT : 2u) : big ? 2u : 1u;
      if (lane < (big ? 2 : 1)) __hip_atomic_fetch_add(fast ? cntf_ptr(p.ws, kind, c) : cnt_ptr(p.ws, kind, L, c + lane), inc, __ATOMIC_RELAXED, __HIP_MEMORY_SCOPE_AGENT);
    }
  }
}

}

extern "C" void kernel_launch(void* const* d_in, const int* in_sizes, int n_in, void* d_out, int out_size, void* d_ws, size_t ws_size,
                              hipStream_t stream) {
  const bf16_t* A_x = (const bf16_t*)d_in[0];
  const bf16_t* A_mem0 = (const bf16_t*)d_in[1];
  const bf16_t* A_Wq = (const bf16_t*)d_in[2];
  const bf16_t* A_Wk = (const bf16_t*)d_in[3];
  const bf16_t* A_Wv = (const bf16_t*)d_in[4];
  const bf16_t* A_Wo = (const bf16_t*)d_in[5];
  const bf16_t* B_w1 = (const bf16_t*)d_in[8];
  const bf16_t* B_b1 = (const bf16_t*)d_in[9];
  const bf16_t* B_w2 = (const bf16_t*)d_in[10];
  const bf16_t* B_b2 = (const bf16_t*)d_in[11];
  unsigned char* ws = (unsigned char*)(((uintptr_t)d_ws + C_PAGE - 1) & ~(uintptr_t)(C_PAGE - 1));
  bf16_t* wqkvT = (bf16_t*)(ws + WS_WQKVT);
  bf16_t* xn = (bf16_t*)(ws + WS_XN);

  {
    PreParams tp;
    int t0 = 0;
    auto add = [&](int i, const bf16_t* s, bf16_t* d, int R, int C) { tp.j[i].src = s; tp.j[i].dst = d; tp.j[i].R = R; tp.j[i].C = C; tp.j[i].tile0 = t0; tp.j[i].pad = 0; t0 += (R / 64) * (C / 64); };
    add(0, A_Wk, wqkvT + (size_t)HD * DM, DM, HD);
    add(1, A_Wv, wqkvT + (size_t)2 * HD * DM, DM, HD);
    tp.x = A_x; tp.xn = xn; tp.ws = ws; tp.ntr = t0; tp.pad = 0;
    hipLaunchKernelGGL(k_pre, dim3(PRE_CAL + t0 + PRE_LN + PRE_FZ + PRE_FF), dim3(256), 0, stream, tp);
  }
  (void)hipFuncSetAttribute((const void*)k_mega, hipFuncAttributeMaxDynamicSharedMemorySize, LDS_TOTAL);
  MegaP mp{A_x, A_mem0, B_b1, B_b2, B_w1, B_w2, A_Wq, A_Wo, (bf16_t*)d_out, ws};
  hipLaunchKernelGGL(k_mega, dim3(256), dim3(512), LDS_TOTAL, stream, mp);
}
```
